```python
import torch

def _softmax(x, axis=-1):
    return torch.softmax(x, dim=axis)
def _gelu(x):
    return torch.nn.functional.gelu(x, approximate="tanh")
def _silu(x):
    return torch.nn.functional.silu(x)
def _sigmoid(x):
    return torch.sigmoid(x)
def _layernorm(x, axis=-1, eps=1e-5):
    m = x.mean(dim=axis, keepdim=True)
    v = x.var(dim=axis, keepdim=True, unbiased=False)
    return (x - m) / torch.sqrt(v + eps)
def _scatter_add(tgt, idx, src, axis=0):
    return tgt.clone().index_add_(axis, idx, src)
def _segment_sum(src, seg, n):
    out = torch.zeros((n,) + src.shape[1:], dtype=src.dtype)
    return out.index_add_(0, seg, src)
def _pad(x, pw):
    flat = tuple(v for pair in reversed(pw) for v in pair)
    return torch.nn.functional.pad(x, flat)

kernel_name = 'recurrent_scan__sliding_block_cached__mixer_token_mlp__decode'

def setup_inputs():

    _w = (lambda a: (lambda t: t.to(torch.bfloat16) if t.is_floating_point() else t)(torch.as_tensor(a)))
    def _A_setup():
        B, Sq, D = 1, 1, 576
        Hq, Hkv, Sk, Dh = 32, 16, 65536, 128
        assert Hq % Hkv == 0, (Hq, Hkv)
        return {
            "x": _w((lambda *s: torch.randn(*s, dtype=torch.bfloat16) * 0.05)(B, Sq, D)),
            "k_cache": _w((lambda *s: torch.randn(*s, dtype=torch.bfloat16) * 3.2)(B, Hkv, Sk, Dh)),
            "v_cache": _w((lambda *s: torch.randn(*s, dtype=torch.bfloat16) * 3.2)(B, Hkv, Sk, Dh)),
            "wq": _w((lambda *s: torch.randn(*s, dtype=torch.bfloat16) * 3.2)(D, Hq * Dh) * (D ** -0.5)),
            "wo": _w((lambda *s: torch.randn(*s, dtype=torch.bfloat16) * 3.2)(Hq * Dh, D) * ((Hq * Dh) ** -0.5)),
        }
    _A_in = _A_setup()

    def _B_setup():
        B, S, D, F = 1, 1, 576, 4608
        return {
            "x": _w((lambda *s: torch.randn(*s, dtype=torch.bfloat16) * 0.05)(B, S, D)),
            "w1": _w((lambda *s: torch.randn(*s, dtype=torch.bfloat16) * 3.2)(S, F) * (S ** -0.5)),
            "b1": _w((lambda *s: torch.randn(*s, dtype=torch.bfloat16) * 3.2)(F) * 0.02),
            "w2": _w((lambda *s: torch.randn(*s, dtype=torch.bfloat16) * 0.4)(F, S) * (F ** -0.5)),
            "b2": _w((lambda *s: torch.randn(*s, dtype=torch.bfloat16) * 0.4)(S) * 0.02),
        }
    _B_in = _B_setup()

    return {
        "A_x": _A_in["x"],
        "A_k_cache": _A_in["k_cache"],
        "A_v_cache": _A_in["v_cache"],
        "A_wq": _A_in["wq"],
        "A_wo": _A_in["wo"],
        "B_x": _B_in["x"],
        "B_w1": _B_in["w1"],
        "B_b1": _B_in["b1"],
        "B_w2": _B_in["w2"],
        "B_b2": _B_in["b2"],
    }

def reference(A_x, A_k_cache, A_v_cache, A_wq, A_wo, B_x, B_w1, B_b1, B_w2, B_b2):
    def _A_ref(x, k_cache, v_cache, wq, wo):
        B, Sq, D = x.shape
        Hq, Dh = 32, 128
        Hkv, Sk = k_cache.shape[1], k_cache.shape[2]
        W = min(4096, max(256, Sk // 4))
        rep = Hq // Hkv
        q = (lambda x, *d: x.permute(*d))((x @ wq).reshape(B, Sq, Hq, Dh), 0, 2, 1, 3)
        k = (lambda x, n, axis: torch.repeat_interleave(x, n, dim=axis))(k_cache[:, :, Sk - W:, :], rep, axis=1)
        v = (lambda x, n, axis: torch.repeat_interleave(x, n, dim=axis))(v_cache[:, :, Sk - W:, :], rep, axis=1)
        scores = torch.einsum("bhqd,bhkd->bhqk", q, k) * (Dh ** -0.5)
        p = _softmax(scores, axis=-1)
        o = torch.einsum("bhqk,bhkd->bhqd", p, v)
        return (lambda x, *d: x.permute(*d))(o, 0, 2, 1, 3).reshape(B, Sq, Hq * Dh) @ wo

    def _B_ref(x, w1, b1, w2, b2):
        B, S, D = x.shape
        h = _layernorm(x, axis=-1)
        u = torch.einsum("bsd,sf->bdf", h, w1) + b1
        u = _gelu(u)
        y = torch.einsum("bdf,fs->bds", u, w2) + b2
        return (lambda x, *d: x.permute(*d))(y, 0, 2, 1)

    x = A_x
    xn = _layernorm(x, axis=-1)
    s = _A_ref(xn, A_k_cache, A_v_cache, A_wq, A_wo)
    g = _silu(_B_ref(xn, B_w1, B_b1, B_w2, B_b2))
    return x + s * g
```

```cpp
#define CODE_BYTES 14784
#define P_BURST 4
#define EXPT_W 3
#define EXPT_I 2
#define EXPT_B 0
#define EXPT_AMP 0
#include <hip/hip_runtime.h>
#include <stdint.h>

#define LAS __attribute__((address_space(3)))
typedef unsigned u32;
typedef unsigned long long u64;
typedef unsigned short u16;
typedef u32 u32x4 __attribute__((ext_vector_type(4)));
typedef u32 u32x2 __attribute__((ext_vector_type(2)));
typedef float f32x4 __attribute__((ext_vector_type(4)));
typedef float f32x2 __attribute__((ext_vector_type(2)));
typedef __bf16 bf16x8 __attribute__((ext_vector_type(8)));
typedef __bf16 bf16x2 __attribute__((ext_vector_type(2)));
typedef short s16x4 __attribute__((ext_vector_type(4)));
typedef LAS char* lptr;
#define GAS __attribute__((address_space(1)))
typedef u64 u64x8 __attribute__((ext_vector_type(8)));
typedef u64 u64x2 __attribute__((ext_vector_type(2)));

#define WOPOL " nt"
#define OUTAUX 0
constexpr int WCAP = 44;
constexpr int QPD = 60;
#define P_PACE (36 * 1024 / 10)
#define P_QT 310
#define P_QF 150
#define P_WOA 40
#define P_WOB 48
#define P_H1D 20
#define P_H2D 70
#define P_MODE 30
#define P_BD 35
#define P_PT 6
#define P_RD 20
#define DBG_ARG
#define DBG_PASS
#define DUE(n) ((u32)(((n) - P_BURST) * P_PACE) >> 10)
constexpr int SK = 65536, WIN = 4096;
constexpr u32 WS_EPOCH = 0;
constexpr u32 WS_ST = 4096;
constexpr u32 WS_C1 = 1024 * 40;
constexpr u32 WS_C2 = 1024 * 56;
constexpr u32 WS_QX = 65536;
constexpr u32 WS_H1 = WS_QX + 16 * 8192;
constexpr u32 WS_Y = WS_H1 + 16 * 32768;
constexpr u32 WS_H2 = WS_Y + 288 * 1024;
constexpr u32 WS_END = WS_H2 + 288 * 1024;
constexpr u32 L_P = 0, L_Q = 1024, L_XN = 1536, L_H = 2688, L_X = 3840, L_G = 4992, L_ML = 5248, L_F = 5312, L_PAR = 5376,
              L_WO = 6400, L_K = 24832, L_V = L_K + 65536, L_END = L_V + 65536;
constexpr u32 F_PROG = L_F, F_XN = L_F + 4, F_H = L_F + 8, F_Q = L_F + 12, F_P = L_F + 16, F_PV = L_F + 20, F_O = L_F + 24, F_TAG = L_F + 28, F_KP = L_F + 32, F_G = L_F + 36, F_SH = L_F + 40, F_B0 = L_F + 48, F_B1 = L_F + 52;
constexpr u32 SPIN_G = 1u << 17, SPIN_L = 1u << 22;
constexpr float LOG2E = 1.4426950408889634f;

__device__ __forceinline__ float asf(u32 x) { return __builtin_bit_cast(float, x); }
__device__ __forceinline__ u32 asu(float x) { return __builtin_bit_cast(u32, x); }
__device__ __forceinline__ float lo16(u32 x) { return asf(x << 16); }
__device__ __forceinline__ float hi16(u32 x) { return asf(x & 0xffff0000u); }
__device__ __forceinline__ u32 pk2(float a, float b) { bf16x2 v = {(__bf16)a, (__bf16)b}; return __builtin_bit_cast(u32, v); }
__device__ __forceinline__ float rb(float a) { return (float)(__bf16)a; }
__device__ __forceinline__ float dot2(u32 a, u32 b, float c) {
  return __builtin_amdgcn_fdot2_f32_bf16(__builtin_bit_cast(bf16x2, a), __builtin_bit_cast(bf16x2, b), c, false);
}
template <int CTRL> __device__ __forceinline__ float dpp(float x) {
  return asf(__builtin_amdgcn_mov_dpp((int)asu(x), CTRL, 0xf, 0xf, true));
}
constexpr int XOR1 = 0xB1, XOR2 = 0x4E, HMIR = 0x141, MIR = 0x140, ROR8 = 0x128;
__device__ __forceinline__ float swap16_sum(float a, float b) {
  auto s = __builtin_amdgcn_permlane16_swap(asu(a), asu(b), false, false);
  return asf(s[0]) + asf(s[1]);
}
__device__ __forceinline__ float swap32_sum(float a, float b) {
  auto s = __builtin_amdgcn_permlane32_swap(asu(a), asu(b), false, false);
  return asf(s[0]) + asf(s[1]);
}
__device__ __forceinline__ float vmax(float a, float b) { float r; asm("v_max_f32 %0, %1, %2" : "=v"(r) : "v"(a), "v"(b)); return r; }
__device__ __forceinline__ float row_sum(float x) {
  x += dpp<XOR1>(x); x += dpp<XOR2>(x); x += dpp<HMIR>(x); x += dpp<MIR>(x); return x;
}
__device__ __forceinline__ void row_max2(float& a, float& b) {
  asm("s_nop 1\n\t"
      "v_max_f32_dpp %0, %0, %0 quad_perm:[1,0,3,2] row_mask:0xf bank_mask:0xf\n\t"
      "v_max_f32_dpp %1, %1, %1 quad_perm:[1,0,3,2] row_mask:0xf bank_mask:0xf\n\ts_nop 0\n\t"
      "v_max_f32_dpp %0, %0, %0 quad_perm:[2,3,0,1] row_mask:0xf bank_mask:0xf\n\t"
      "v_max_f32_dpp %1, %1, %1 quad_perm:[2,3,0,1] row_mask:0xf bank_mask:0xf\n\ts_nop 0\n\t"
      "v_max_f32_dpp %0, %0, %0 row_half_mirror row_mask:0xf bank_mask:0xf\n\t"
      "v_max_f32_dpp %1, %1, %1 row_half_mirror row_mask:0xf bank_mask:0xf\n\ts_nop 0\n\t"
      "v_max_f32_dpp %0, %0, %0 row_mirror row_mask:0xf bank_mask:0xf\n\t"
      "v_max_f32_dpp %1, %1, %1 row_mirror row_mask:0xf bank_mask:0xf\n\ts_nop 1"
      : "+v"(a), "+v"(b));
}
__device__ __forceinline__ float xrow_max(float a) {
  auto s = __builtin_amdgcn_permlane16_swap(asu(a), asu(a), false, false);
  a = vmax(asf(s[0]), asf(s[1]));
  asm("s_nop 1" : "+v"(a));
  auto t = __builtin_amdgcn_permlane32_swap(asu(a), asu(a), false, false);
  return vmax(asf(t[0]), asf(t[1]));
}
__device__ __forceinline__ float wave_sum(float x) { x = row_sum(x); x = swap16_sum(x, x); return swap32_sum(x, x); }
__device__ __forceinline__ f32x4 mfma(u32x4 a, u32x4 b, f32x4 c) {
  return __builtin_amdgcn_mfma_f32_16x16x32_bf16(__builtin_bit_cast(bf16x8, a), __builtin_bit_cast(bf16x8, b), c, 0, 0, 0);
}
__device__ __forceinline__ u32x4 cat(s16x4 a, s16x4 b) { return __builtin_bit_cast(u32x4, __builtin_shufflevector(a, b, 0, 1, 2, 3, 4, 5, 6, 7)); }
__device__ __forceinline__ s16x4 trd(lptr p) { return __builtin_amdgcn_ds_read_tr16_b64_v4i16((LAS s16x4*)p); }

#define CBAR() asm volatile("" ::: "memory")
#define SBAR() __builtin_amdgcn_sched_barrier(0)
template <typename T> __device__ __forceinline__ T lds_ld(lptr p) { return *(const LAS T*)p; }
template <typename T> __device__ __forceinline__ void lds_st(lptr p, T v) { *(LAS T*)p = v; }
__device__ __forceinline__ u32 flag_ld(lptr p) { return __builtin_amdgcn_readfirstlane(*(volatile LAS u32*)p); }
__device__ __forceinline__ void flag_st(lptr p, u32 v) { CBAR(); *(volatile LAS u32*)p = v; CBAR(); }
__device__ __forceinline__ void flag_add(u32 a) {
  u64 sv;
  asm volatile("s_mov_b64 %0, exec\n\ts_mov_b64 exec, 1\n\tds_add_u32 %1, %2\n\ts_mov_b64 exec, %0" : "=&s"(sv) : "v"(a), "v"(1u) : "memory");
}
template <int SLEEP> __device__ __forceinline__ void flag_wait(lptr p, u32 v) {
  u32 t, n;
  asm volatile("s_mov_b32 %1, %5\n"
               "1:\n\tds_read_b32 %0, %2\n\ts_waitcnt lgkmcnt(0)\n\tv_cmp_le_u32 vcc, %3, %0\n\ts_cbranch_vccnz 2f\n\t"
               "s_sleep %4\n\ts_sub_u32 %1, %1, 1\n\ts_cbranch_scc0 1b\n2:"
               : "=&v"(t), "=&s"(n) : "v"(p), "s"(v), "i"(SLEEP), "i"(SPIN_L) : "vcc", "scc", "memory");
}
__device__ __forceinline__ u32 now() {
  u64 t;
  asm volatile("s_memrealtime %0\n\ts_waitcnt lgkmcnt(0)" : "=s"(t)::"memory");
  return (u32)t;
}
template <int SLEEP> __device__ __forceinline__ void wait_until(u32 due) {
  u32 n;
  asm volatile("s_movk_i32 %0, 0x1000\n"
               "1:\n\ts_memrealtime s[60:61]\n\ts_waitcnt lgkmcnt(0)\n\ts_sub_u32 s60, s60, %1\n\ts_cmp_ge_i32 s60, 0\n\ts_cbranch_scc1 2f\n\t"
               "s_sleep %2\n\ts_sub_u32 %0, %0, 1\n\ts_cbranch_scc0 1b\n2:"
               : "=&s"(n) : "s"(due), "i"(SLEEP) : "s60", "s61", "scc", "memory");
}

typedef __amdgpu_buffer_rsrc_t rsrc_t;
__device__ __forceinline__ u32x4 ld_unit(rsrc_t r, u32 voff, u32 soff) { return __builtin_amdgcn_raw_buffer_load_b128(r, voff, soff, 16); }
__device__ __forceinline__ void st_unit(rsrc_t r, u32 voff, u32 soff, float a, float b, u32 tag) {
  u32x4 v = {asu(a), tag, asu(b), tag};
  __builtin_amdgcn_raw_buffer_store_b128(v, r, voff, soff, 16);
}
__device__ __forceinline__ void sweep8(u32x4 (&u)[8], rsrc_t r, u32 voff, u32 soff) {
  CBAR();
#pragma unroll
  for (int i = 0; i < 8; ++i) u[i] = ld_unit(r, voff + (i & 3) * 1024, soff + (i >> 2) * 4096);
}
__device__ __forceinline__ u32 bad_tags(u32 d, u32x4 u, u32 tag) {
  d = __builtin_amdgcn_bitop3_b32(u.y, tag, d, 0xBE);
  return __builtin_amdgcn_bitop3_b32(u.w, tag, d, 0xBE);
}
__device__ __forceinline__ bool ok8(const u32x4 (&u)[8], u32 d, u32 tag) {
  u32 e = 0;
#pragma unroll
  for (int i = 0; i < 8; ++i) {
    d = __builtin_amdgcn_bitop3_b32(u[i].y, tag, d, 0xBE);
    e = __builtin_amdgcn_bitop3_b32(u[i].w, tag, e, 0xBE);
  }
  return __builtin_amdgcn_ballot_w64((d | e) != 0) == 0;
}
template <int MASK> __device__ __forceinline__ void hint_add(rsrc_t r, u32 voff, u32 val) {
  u64 sv;
  asm volatile("s_mov_b64 %0, exec\n\ts_mov_b64 exec, %4\n\tbuffer_atomic_add %1, %2, %3, 0 offen\n\ts_mov_b64 exec, %0"
               : "=&s"(sv) : "v"(val), "v"(voff), "s"(r), "i"(MASK) : "memory");
}
template <int MASK, int SHIFT> __device__ __forceinline__ void hint_wait(rsrc_t r, u32 voff, u32 soff, u32 base) {
  u64 sv;
  u32 t, n;
  asm volatile("s_mov_b64 %0, exec\n\ts_mov_b64 exec, %7\n\ts_mov_b32 %2, 23\n"
               "1:\n\tv_mov_b32 %1, 0\n\tbuffer_atomic_add %1, %3, %4, %5 offen sc0\n\ts_waitcnt vmcnt(0)\n\t"
               "v_sub_u32 %1, %1, %6\n\tv_bfe_u32 %1, %1, %8, 16\n\tv_cmp_gt_u32 vcc, 16, %1\n\ts_cbranch_vccz 2f\n\ts_sub_u32 %2, %2, 1\n\ts_cbranch_scc0 1b\n"
               "2:\n\ts_mov_b64 exec, %0"
               : "=&s"(sv), "=&v"(t), "=&s"(n) : "v"(voff), "s"(r), "s"(soff), "v"(base), "i"(MASK), "i"(SHIFT) : "vcc", "scc", "memory");
}

__device__ __forceinline__ u32 hint_wait_time(rsrc_t r, u32 voff, u32 soff, u32 base) {
  u64 sv;
  u32 t, n, tm;
  asm volatile("s_mov_b64 %0, exec\n\ts_mov_b64 exec, 15\n\ts_mov_b32 %2, 23\n"
               "1:\n\tv_mov_b64 v[200:201], 0\n\tbuffer_atomic_add_x2 v[200:201], %4, %5, %6 offen sc0\n\ts_waitcnt vmcnt(0)\n\t"
               "v_sub_u32 %1, v200, %7\n\tv_lshrrev_b32 %1, 16, %1\n\tv_cmp_gt_u32 vcc, 16, %1\n\ts_cbranch_vccz 2f\n\ts_sub_u32 %2, %2, 1\n\ts_cbranch_scc0 1b\n"
               "2:\n\tv_max_u32_dpp v201, v201, v201 quad_perm:[1,0,3,2] row_mask:0xf bank_mask:0xf\n\ts_nop 1\n\t"
               "v_max_u32_dpp v201, v201, v201 quad_perm:[2,3,0,1] row_mask:0xf bank_mask:0xf\n\ts_nop 0\n\t"
               "v_readfirstlane_b32 %3, v201\n\ts_mov_b64 exec, %0"
               : "=&s"(sv), "=&v"(t), "=&s"(n), "=&s"(tm) : "v"(voff), "s"(r), "s"(soff), "v"(base) : "v200", "v201", "vcc", "scc", "memory");
  return tm;
}

__device__ __forceinline__ u64 rt() { u64 t; SBAR(); asm volatile("s_memrealtime %0\n\ts_waitcnt lgkmcnt(0)" : "=s"(t)::"memory"); SBAR(); return t; }
#define STAMP(i) do { if ((i) == 0) ts0 = rt(); if ((i) == EXPT_I && wave == EXPT_W) ts1 = rt(); } while (0)
#define STAMP0() asm volatile("s_memrealtime %0" : "=s"(ts0))
#define STAMP_DECL u64 ts0 = 0, ts1 = 0
#define STAMP_OUT() do { if (wave == EXPT_W && ts1 && (EXPT_B < 0 || b == EXPT_B)) { const u64 lim = ts0 + 4000 + (ts1 - ts0) * EXPT_AMP; for (int n_ = 0; n_ < 1000000; ++n_) { if (rt() >= lim) break; __builtin_amdgcn_s_sleep(4); } } } while (0)

__device__ __forceinline__ void dma4(u32 ldsaddr, const char* base, u32 o0, u32 o1, u32 o2, u32 o3) {
  asm volatile(
      "s_mov_b32 m0, %0\n\ts_nop 0\n\t"
      "global_load_lds_dwordx4 %1, %5 nt\n\t"
      "global_load_lds_dwordx4 %2, %5 offset:1024 nt\n\t"
      "global_load_lds_dwordx4 %3, %5 offset:2048 nt\n\t"
      "global_load_lds_dwordx4 %4, %5 offset:3072 nt"
      ::"s"(ldsaddr), "v"(o0), "v"(o1), "v"(o2), "v"(o3), "s"(base) : "memory");
}
__device__ __forceinline__ void dma1(u32 ldsaddr, const char* base, u32 o0) {
  asm volatile("s_mov_b32 m0, %0\n\ts_nop 0\n\tglobal_load_lds_dwordx4 %1, %2" WOPOL ::"s"(ldsaddr), "v"(o0), "s"(base) : "memory");
}
template <int N> __device__ __forceinline__ void prog_pub(u32 fa, int landed) {
  asm volatile("s_waitcnt vmcnt(%2)\n\tds_write_b32 %0, %1" ::"v"(fa), "v"(landed), "i"(N) : "memory");
}
template <int T, int STEP> __device__ __forceinline__ void drain(u32 fa) {
  if constexpr (T <= 64) {
    if constexpr (64 - T < WCAP) prog_pub<64 - T>(fa, T);
    drain<T + STEP, STEP>(fa);
  }
}
__device__ __forceinline__ void load_tile(int FIRST, u32 lds, const char* base, int g, u32 o0, u32 x1, u32 t1, u32 fa DBG_ARG) {
  const u32 o1 = o0 ^ x1, o2 = o0 ^ (2 * x1), o3 = o0 ^ (3 * x1);
#pragma nounroll
  for (int i = 0; i < 16; ++i) {
    wait_until<0>(t1 + DUE(FIRST + 4 * i));
    dma4(lds + i * 4096, base + (((g + i) & 15) << 12), o0, o1, o2, o3);
    prog_pub<WCAP>(fa, 4 * i + 4 > WCAP ? 4 * i + 4 - WCAP : 0);
  }
}

__device__ __forceinline__ void ln9(const float (&v)[9], float (&o)[9]) {
  float s = ((v[0] + v[1]) + (v[2] + v[3])) + ((v[4] + v[5]) + (v[6] + v[7])) + v[8];
  const float mean = wave_sum(s) * (1.0f / 576.0f);
  float q = 0.f, q2 = 0.f;
#pragma unroll
  for (int i = 0; i < 9; ++i) { const float d = v[i] - mean; if (i & 1) q2 = fmaf(d, d, q2); else q = fmaf(d, d, q); }
  const float var = wave_sum(q + q2) * (1.0f / 576.0f);
  const float mb = rb(mean);
  const float den = rb(__builtin_amdgcn_sqrtf(rb(rb(var) + 1e-5f)));
  const float inv = __builtin_amdgcn_rcpf(den);
#pragma unroll
  for (int i = 0; i < 9; ++i) o[i] = rb(rb(v[i] - mb) * inv);
}
__device__ __forceinline__ float gate_tile(float hh, const u32 (&p1)[9], const u32 (&pb)[9], const u32 (&p2)[9]) {
  constexpr float C0 = -2.0f * 0.7978845608028654f * LOG2E, C1 = C0 * 0.044715f;
  float acc = 0.f;
#pragma unroll
  for (int j = 0; j < 9; ++j) {
    f32x2 m = f32x2{lo16(p1[j]), hi16(p1[j])} * hh;
    u32 r = pk2(m.x, m.y);
    m = f32x2{lo16(r), hi16(r)} + f32x2{lo16(pb[j]), hi16(pb[j])};
    r = pk2(m.x, m.y);
    const f32x2 uu = {lo16(r), hi16(r)};
    const f32x2 z = uu * (uu * uu * C1 + C0);
    const f32x2 dn = f32x2{__builtin_amdgcn_exp2f(z.x), __builtin_amdgcn_exp2f(z.y)} + 1.0f;
    const f32x2 gl = uu * f32x2{__builtin_amdgcn_rcpf(dn.x), __builtin_amdgcn_rcpf(dn.y)};
    acc = dot2(pk2(gl.x, gl.y), p2[j], acc);
  }
  acc = swap16_sum(acc, acc);
  return swap32_sum(acc, acc);
}
__device__ __forceinline__ void gate_par(lptr L, int kg, u32 (&p1)[9], u32 (&pb)[9], u32 (&p2)[9]) {
#pragma unroll
  for (int j = 0; j < 9; ++j) {
    const lptr pa = L + L_PAR + kg * 36 + j * 4;
    p1[j] = lds_ld<u32>(pa); pb[j] = lds_ld<u32>(pa + 256); p2[j] = lds_ld<u32>(pa + 512);
  }
}
template <int KS> __device__ __forceinline__ void v_step(u32x4& b0, u32x4& b1, const lptr (&va)[2][2], lptr prog) {
  u32x2 x0, x1, x2, x3;
  u32 t, n;
  asm volatile("s_mov_b32 %5, %12\n"
               "1:\n\tds_read_b32 %4, %10\n\t"
               "ds_read_b64_tr_b16 %0, %6 offset:%11\n\tds_read_b64_tr_b16 %1, %7 offset:%11\n\t"
               "ds_read_b64_tr_b16 %2, %8 offset:%11\n\tds_read_b64_tr_b16 %3, %9 offset:%11\n\t"
               "s_waitcnt lgkmcnt(0)\n\tv_cmp_le_u32 vcc, %13, %4\n\ts_cbranch_vccnz 2f\n\ts_sub_u32 %5, %5, 1\n\ts_cbranch_scc0 1b\n2:"
               : "=&v"(x0), "=&v"(x1), "=&v"(x2), "=&v"(x3), "=&v"(t), "=&s"(n)
               : "v"(va[0][0]), "v"(va[0][1]), "v"(va[1][0]), "v"(va[1][1]), "v"(prog), "i"(KS * 8192), "i"(SPIN_L), "i"(8 + 8 * KS)
               : "vcc", "scc", "memory");
  b0 = u32x4{x0.x, x0.y, x1.x, x1.y};
  b1 = u32x4{x2.x, x2.y, x3.x, x3.y};
}
__device__ __forceinline__ void q_wait(u32x4 (&q)[4], lptr qa, lptr flag) {
  u32 t, n;
  asm volatile("s_mov_b32 %5, %8\n"
               "1:\n\tds_read_b32 %4, %7\n\tds_read_b128 %0, %6\n\tds_read_b128 %1, %6 offset:64\n\tds_read_b128 %2, %6 offset:128\n\tds_read_b128 %3, %6 offset:192\n\t"
               "s_waitcnt lgkmcnt(0)\n\tv_cmp_le_u32 vcc, 1, %4\n\ts_cbranch_vccnz 2f\n\ts_sub_u32 %5, %5, 1\n\ts_cbranch_scc0 1b\n2:"
               : "=&v"(q[0]), "=&v"(q[1]), "=&v"(q[2]), "=&v"(q[3]), "=&v"(t), "=&s"(n) : "v"(qa), "v"(flag), "i"(SPIN_L) : "vcc", "scc", "memory");
}
__device__ __forceinline__ float sum64(const u32x4 (&u)[8]) {
  const float a0 = ((asf(u[0].x) + asf(u[1].x)) + (asf(u[2].x) + asf(u[3].x))) + ((asf(u[4].x) + asf(u[5].x)) + (asf(u[6].x) + asf(u[7].x)));
  const float a1 = ((asf(u[0].z) + asf(u[1].z)) + (asf(u[2].z) + asf(u[3].z))) + ((asf(u[4].z) + asf(u[5].z)) + (asf(u[6].z) + asf(u[7].z)));
  float tot = swap32_sum(a0, a1);
  tot = swap16_sum(tot, tot);
  return rb(tot + dpp<ROR8>(tot));
}
__device__ __forceinline__ float gate_of(float s, float b2f) {
  const float y = rb(s + b2f);
  return rb(y * __builtin_amdgcn_rcpf(1.0f + __builtin_amdgcn_exp2f(-LOG2E * y)));
}
__device__ __forceinline__ float sel4(int kg, float a, float b, float c, float d) {
  const float lo = (kg & 1) ? b : a, hi = (kg & 1) ? d : c;
  return (kg & 2) ? hi : lo;
}

struct Touch {
  const void* p[16];
};

__global__ void __launch_bounds__(512, 2)
fused_decode(const u16* a_wq, const u16* a_x, const u16* a_w1, const u16* a_b1, const u16* a_w2, const u16* a_kc, const u16* a_vc,
             const u16* a_wo, const u16* a_b2, u16* a_out, char* a_ws, Touch a_touch) {
  __shared__ __attribute__((aligned(16))) char smem[L_END];
  const lptr L = (lptr)smem;
  const u32 LB = (u32)(uintptr_t)L;
  const int b = blockIdx.x, lane = threadIdx.x & 63, wave = __builtin_amdgcn_readfirstlane(threadIdx.x >> 6);
  u64x8 A;
  u64x2 B;
  u64 C;
  asm volatile("s_cmp_lg_u32 %4, 7\n\ts_cbranch_scc1 1f\n\tv_mov_b32 v200, 0\n\tv_and_b32 v201, 15, %5\n\tv_lshlrev_b32 v201, 2, v201\n\t"
               "ds_write_b32 v201, v200 offset:%6\n\ts_waitcnt lgkmcnt(0)\n1:\n\t"
               "s_load_dwordx16 %0, %3, 0x0\n\ts_load_dwordx4 %1, %3, 0x40\n\ts_load_dwordx2 %2, %3, 0x50"
               : "=&s"(A), "=&s"(B), "=&s"(C) : "s"(__builtin_amdgcn_kernarg_segment_ptr()), "s"(wave), "v"(threadIdx.x), "i"(L_F)
               : "v200", "v201", "scc", "memory");
  SBAR();
  STAMP_DECL;
  STAMP0();
  const int g = (b & 7) * 2 + (b >> 7), s = (b >> 3) & 15, rbk = s >> 2, cb = s & 3, pi = g * 4 + rbk;
  const int n16 = lane & 15, kg = lane >> 4, r8 = lane >> 3;
  u64 tp;
  u32 dump;
  asm volatile("s_cmp_lt_u32 %4, 4\n\ts_cbranch_scc1 2f\n\ts_cmp_lg_u32 %4, 4\n\ts_cbranch_scc1 1f\n\t"
               "s_load_dwordx2 %0, %2, %3 offset:0x58\n\ts_branch 2f\n"
               "1:\n\ts_getpc_b64 %0\n\ts_andn2_b64 %0, %0, 0xff\n\ts_load_dword %1, %0, %5\n\ts_load_dword %1, %0, %5 offset:64\n\ts_load_dword %1, %0, %5 offset:128\n2:"
               : "=&s"(tp), "=&s"(dump)
               : "s"(__builtin_amdgcn_kernarg_segment_ptr()), "s"(s * 8), "s"(wave),
                 "s"(min((u32)(((b >> 3) * 3 + (wave - 5)) * 192), (u32)(CODE_BYTES - 192))) : "scc");
  const u32 wof = (rbk * 144 + r8 * 18) * 8192 + (g * 256 + cb * 64) * 2 + (lane & 7) * 16;
  const int cbu = b < 36 ? b / 9 : 0;
  const u32 c2l = WS_C2 + cbu * 1024 + lane * 256, c1l = WS_C1 + g * 1024 + rbk * 256;
  u32 epo = lane == 5 ? c1l : WS_EPOCH;
  epo = lane < (b < 36 ? 4 : 0) ? c2l : epo;
  const u32 pof = pi * 144 + n16 * 16;
  const u32 toff = g * (s < 4 ? (u32)SK * 256 : s < 6 ? 256u * 1152 : 0u);
  asm volatile("s_barrier\n\ts_waitcnt lgkmcnt(0)"
               : "+s"(A), "+s"(B), "+s"(C), "+s"(tp), "+s"(dump)
               : "s"(wave), "v"(wof), "v"(lane * 16), "s"(toff), "v"(epo), "v"(pof), "v"(lane * 2) : "memory");
  u64 t64;
  asm volatile("s_memrealtime %0" : "=s"(t64));
  SBAR();
  u32x4 w[18], xa, par;
  u32 xb, ep, b2v;
  if (wave == 2) {
    asm volatile("global_load_dwordx4 %0, %5, %9\n\tglobal_load_ushort %1, %6, %9 offset:1024\n\tglobal_load_dword %2, %7, %10 sc1\n\t"
                 "s_mov_b64 exec, 0x1ff\n\tglobal_load_dwordx4 %3, %8, %11\n\t"
                 "s_mov_b64 exec, 0x1ff0000\n\tglobal_load_dwordx4 %3, %8, %12\n\t"
                 "s_lshl_b64 exec, exec, 16\n\tglobal_load_dwordx4 %3, %8, %13\n\t"
                 "s_mov_b64 exec, %15\n\tglobal_load_ushort %4, %6, %14\n\ts_mov_b64 exec, -1"
                 : "=&v"(xa), "=&v"(xb), "=&v"(ep), "=&v"(par), "=&v"(b2v)
                 : "v"(lane * 16), "v"(lane * 2), "v"(epo), "v"(pof), "s"(A[1]), "s"(C), "s"(A[2]), "s"(A[3]), "s"(A[4]), "s"(b < 36 ? B[0] : A[1]), "s"(1ull)
                 : "scc", "memory");
  }
  if (wave == 4 && (s >= 6 || s < P_PT)) {
    asm volatile("s_load_dword %0, %1, 0x0" : "=&s"(dump) : "s"(tp + toff));
    }
  if (wave == 1) {
    const rsrc_t wqr = __builtin_amdgcn_make_buffer_rsrc((void*)A[0], 0, 576 * 8192, 0x00020000);
#pragma unroll
    for (int j = 0; j < 18; ++j) { w[j] = __builtin_amdgcn_raw_buffer_load_b128(wqr, wof, j * 8192, 2); SBAR(); }
  }
  SBAR();
  char* const ws = (char*)C;
  const rsrc_t wsr = __builtin_amdgcn_make_buffer_rsrc(ws, 0, WS_END, 0x00020000);
  size_t kvoff = ((size_t)g * SK + (SK - WIN) + s * 256) * 256;
  const char* wb = (const char*)(A[7] + ((g * 256 + rbk * 32) * 576 + cb * 144) * 2);
  asm volatile("s_waitcnt lgkmcnt(0)" : "+s"(t64), "+s"(dump));
  const u32 t1 = (u32)t64;
  if (wave == 4) {
    wait_until<2>(t1 + P_MODE);
    u32 d0, d1, d2, d3;
    asm volatile("s_load_dword %0, %4, 0x0\n\ts_load_dword %1, %5, 0x0\n\ts_load_dword %2, %6, 0x0\n\ts_load_dword %3, %6, 0x24000\n\ts_waitcnt lgkmcnt(0)"
                 : "=&s"(d0), "=&s"(d1), "=&s"(d2), "=&s"(d3) : "s"(tp + toff), "s"(A[6] + kvoff), "s"(wb));
  }
  SBAR();
  STAMP(1);

  if (wave == 0 || wave == 3) {
    const bool isv = wave == 0;
    const u32 fa = LB + (isv ? F_PROG : F_KP);
    if (isv) {
      wait_until<8>(t1 + DUE(82) - 30);
    }
    __builtin_amdgcn_s_setprio(3);
    load_tile(isv ? 82 : 18, LB + (isv ? L_V : L_K), (const char*)((isv ? A[6] : A[5]) + kvoff), g, kg * 256 + ((n16 ^ (isv ? kg << 2 : kg)) << 4), isv ? 16 : 64, t1, fa DBG_PASS);
    STAMP(3);
    u32 hv1 = lane < 4 ? WS_C1 + g * 1024 + lane * 256 : WS_C2 + cb * 1024 + (g & 3) * 256;
    asm volatile("" : "+v"(hv1));
    drain<4, 4>(fa);
    if (isv) {
      flag_wait<0>(L + F_PV, 4);
      hint_add<31>(wsr, hv1, 1u);
    }
  }
  asm volatile("" ::: "memory");
  if (wave == 0 || wave == 3) {
    if (wave == 0) {
      STAMP(5);
    const u32 c2o = WS_C2 + cb * 1024 + (g & 3) * 256;
      flag_wait<0>(L + F_O, 1);
      {
        u64 sv;
        asm volatile("s_mov_b64 %0, exec\n\ts_mov_b64 exec, 1\n\tbuffer_atomic_umax %1, off, %3, %4 offset:4\n\tbuffer_atomic_add %2, off, %3, %4\n\ts_mov_b64 exec, %0"
                     : "=&s"(sv) : "v"(now() + P_H2D), "v"(0x10000u), "s"(wsr), "s"(c2o) : "memory");
      }
      STAMP(4);
      STAMP_OUT();
      return;
    }
    STAMP(2);
    flag_wait<2>(L + F_XN, 2);
    const u32 tag = flag_ld(L + F_TAG), hbase = flag_ld(L + F_B1);
    u32 wov[9];
#pragma unroll
    for (int i = 0; i < 9; ++i) {
      const u32 e = i * 64 + lane, rs = (e * 3641) >> 16;
      wov[i] = (e - rs * 18) * 16 + ((rs & 16) | ((rs & 3) << 2) | ((rs >> 2) & 3)) * 1152;
      asm volatile("" : "+v"(wov[i]));
    }
    const u32 hb = WS_H1 + g * 32768 + rbk * 8192, hv = n16 * 512 + kg * 16, sb = WS_ST + g * 2048;
    const u32 h2v = (lane >> 3) * 8192 + (lane & 7) * 16, h2s = WS_H2 + cb * 9 * 8192 + pi * 128;
    const u32 c1s = WS_C1 + g * 1024 + rbk * 256, h8v = lane < 8 ? lane * 16 : 0xfffffff0u;
    const lptr wa = L + L_WO + (4 * kg + (n16 >> 2)) * 288 + (lane & 3) * 8;
    asm volatile("" ::"s"(hb), "s"(sb), "s"(h2s), "s"(c1s), "v"(hv), "v"(h2v), "v"(wa));
    u32x4 s0, s1;
    flag_wait<2>(L + F_PROG, 20);
#pragma nounroll
    for (int hf = 0; hf < 2; ++hf) {
      wait_until<0>(t1 + DUE(146 + 9 * hf) + P_BD);
      flag_wait<0>(L + F_PROG, hf ? P_WOB : P_WOA);
#pragma unroll
      for (int i = 0; i < 9; ++i) dma1(LB + L_WO + (hf * 9 + i) * 1024, wb + hf * (128 * 1152), wov[i]);
    }
    flag_wait<0>(L + F_PROG, 64);
    wait_until<0>(now() + P_H1D);
    asm volatile("buffer_load_dwordx4 %0, %2, %3, %4 offen sc1\n\tbuffer_load_dwordx4 %1, %2, %3, %4 offen offset:16 sc1"
                 : "=&v"(s0), "=&v"(s1) : "v"(n16 * 128), "s"(wsr), "s"(sb) : "memory");
    hint_wait<1, 0>(wsr, 0u, c1s, hbase);
    asm volatile("" : "+v"(s0), "+v"(s1));
    u64 reload = __builtin_amdgcn_ballot_w64(bad_tags(bad_tags(0u, s0, tag), s1, tag) != 0);
#pragma nounroll
    for (u32 it = 0; it < SPIN_G; ++it) {
      STAMP(4);
      u32x4 u[8];
      asm volatile("s_cmp_eq_u64 %10, 0\n\ts_cbranch_scc1 1f\n\t"
                   "buffer_load_dwordx4 %8, %11, %13, %14 offen sc1\n\tbuffer_load_dwordx4 %9, %11, %13, %14 offen offset:16 sc1\n1:\n\t"
                   "buffer_load_dwordx4 %0, %12, %13, %15 offen sc1\n\tbuffer_load_dwordx4 %1, %12, %13, %15 offen offset:64 sc1\n\t"
                   "buffer_load_dwordx4 %2, %12, %13, %15 offen offset:128 sc1\n\tbuffer_load_dwordx4 %3, %12, %13, %15 offen offset:192 sc1\n\t"
                   "buffer_load_dwordx4 %4, %12, %13, %15 offen offset:256 sc1\n\tbuffer_load_dwordx4 %5, %12, %13, %15 offen offset:320 sc1\n\t"
                   "buffer_load_dwordx4 %6, %12, %13, %15 offen offset:384 sc1\n\tbuffer_load_dwordx4 %7, %12, %13, %15 offen offset:448 sc1"
                   : "=&v"(u[0]), "=&v"(u[1]), "=&v"(u[2]), "=&v"(u[3]), "=&v"(u[4]), "=&v"(u[5]), "=&v"(u[6]), "=&v"(u[7]), "+v"(s0), "+v"(s1)
                   : "s"(reload), "v"(n16 * 128), "v"(hv), "s"(wsr), "s"(sb), "s"(hb) : "scc", "memory");
      flag_st(L + F_O, 1);
      SBAR();
      u32x4 bw[9][2];
#pragma unroll
      for (int nt = 0; nt < 9; ++nt) bw[nt][0] = cat(trd(wa + nt * 32), trd(wa + 16 * 288 + nt * 32));
#pragma unroll
      for (int nt = 0; nt < 9; ++nt) bw[nt][1] = cat(trd(wa + 32 * 288 + nt * 32), trd(wa + 48 * 288 + nt * 32));
      SBAR();
      asm volatile("s_cmp_eq_u64 %2, 0\n\ts_cbranch_scc1 1f\n\ts_waitcnt vmcnt(8)\n1:" : "+v"(s0), "+v"(s1) : "s"(reload) : "scc", "memory");
      float M0 = asf(s0.x), M1 = asf(s1.x);
      row_max2(M0, M1);
      const float e0 = __builtin_amdgcn_exp2f((asf(s0.x) - M0) * LOG2E), e1 = __builtin_amdgcn_exp2f((asf(s1.x) - M1) * LOG2E);
      float w0 = e0 * __builtin_amdgcn_rcpf(row_sum(e0 * asf(s0.z)));
      float w1v = e1 * __builtin_amdgcn_rcpf(row_sum(e1 * asf(s1.z)));
      u32 sbad = bad_tags(bad_tags(0u, s0, tag), s1, tag);
      asm volatile("" : "+v"(w0), "+v"(w1v), "+v"(sbad));
      reload = 1;
      STAMP(5);
      SBAR();
      u32x4 a0, a1;
#pragma unroll
      for (int j = 0; j < 4; ++j) {
        if (j == 0) asm volatile("s_waitcnt vmcnt(6)" : "+v"(u[0]), "+v"(u[1])::"memory");
        if (j == 1) asm volatile("s_waitcnt vmcnt(4)" : "+v"(u[2]), "+v"(u[3])::"memory");
        if (j == 2) asm volatile("s_waitcnt vmcnt(2)" : "+v"(u[4]), "+v"(u[5])::"memory");
        if (j == 3) asm volatile("s_waitcnt vmcnt(0)" : "+v"(u[6]), "+v"(u[7])::"memory");
        a0[j] = pk2(w0 * asf(u[2 * j].x), w0 * asf(u[2 * j + 1].x));
        SBAR();
      }
      STAMP(6);
      constexpr int ord[9] = {0, 4, 2, 6, 1, 5, 3, 7, 8};
      f32x4 c[9];
      u32 e2 = 0;
#pragma unroll
      for (int k = 0; k < 9; ++k) {
        c[ord[k]] = mfma(a0, bw[ord[k]][0], f32x4{0.f, 0.f, 0.f, 0.f});
        if (k < 4) a1[k] = pk2(w1v * asf(u[2 * k].z), w1v * asf(u[2 * k + 1].z));
        else if (k < 8) {
          const int j = 2 * (k - 4);
          sbad = __builtin_amdgcn_bitop3_b32(u[j].y, tag, sbad, 0xBE); e2 = __builtin_amdgcn_bitop3_b32(u[j].w, tag, e2, 0xBE);
          sbad = __builtin_amdgcn_bitop3_b32(u[j + 1].y, tag, sbad, 0xBE); e2 = __builtin_amdgcn_bitop3_b32(u[j + 1].w, tag, e2, 0xBE);
        }
        SBAR();
      }
      const bool ok = __builtin_amdgcn_ballot_w64((sbad | e2) != 0) == 0;
      const rsrc_t wr = __builtin_amdgcn_make_buffer_rsrc(ws, 0, ok ? WS_END : 0u, 0x00020000);
      float sm[9], p04 = 0.f, p26 = 0.f, p15 = 0.f, v0 = 0.f;
#pragma unroll
      for (int k = 0; k < 12; ++k) {
        if (k < 9) c[ord[k]] = mfma(a1, bw[ord[k]][1], c[ord[k]]);
        if (k >= 3) { const int nt = ord[k - 3]; const f32x2 t = f32x2{c[nt][0], c[nt][1]} + f32x2{c[nt][2], c[nt][3]}; sm[nt] = t.x + t.y; }
        if (k == 5) p04 = swap32_sum(sm[0], sm[4]);
        if (k == 7) p26 = swap32_sum(sm[2], sm[6]);
        if (k == 8) v0 = swap16_sum(p04, p26);
        if (k == 9) p15 = swap32_sum(sm[1], sm[5]);
        SBAR();
      }
      const float p37 = swap32_sum(sm[3], sm[7]);
      float d8 = swap32_sum(sm[8], sm[8]);
      const float v1 = swap16_sum(p15, p37);
      d8 = swap16_sum(d8, d8);
      st_unit(wr, h2v, h2s, v0, v1, tag);
      st_unit(wr, h8v, h2s + 8 * 8192, d8, dpp<ROR8>(d8), tag);
      if (ok) break;
      asm volatile("s_waitcnt vmcnt(0)" ::: "memory");
    }
    STAMP(7);
    STAMP_OUT();
    return;
  }

  if (wave == 1) {
    __builtin_amdgcn_s_setprio(3);
    asm volatile("s_waitcnt vmcnt(17)" ::: "memory");
    const u32 tf = now();
    STAMP(2);
    flag_wait<0>(L + F_XN, 1);
    u32 xp[9];
#pragma unroll
    for (int i = 0; i < 9; ++i) xp[i] = lds_ld<u32>(L + L_XN + rbk * 288 + r8 * 36 + i * 4);
    float acc[8];
#pragma unroll
    for (int i = 0; i < 8; ++i) acc[i] = 0.f;
#pragma unroll
    for (int i = 0; i < 9; ++i)
#pragma unroll
      for (int d = 0; d < 4; ++d) {
        const u32 a = w[2 * i][d], c = w[2 * i + 1][d];
        acc[2 * d] = dot2(__builtin_amdgcn_perm(c, a, 0x05040100), xp[i], acc[2 * d]);
        acc[2 * d + 1] = dot2(__builtin_amdgcn_perm(c, a, 0x07060302), xp[i], acc[2 * d + 1]);
      }
    float t4[4], t2[2];
#pragma unroll
    for (int i = 0; i < 4; ++i) t4[i] = swap32_sum(acc[i], acc[i + 4]);
#pragma unroll
    for (int i = 0; i < 2; ++i) { t2[i] = swap16_sum(t4[i], t4[i + 2]); t2[i] += dpp<ROR8>(t2[i]); }
    const u32 qxb = WS_QX + g * 8192;
    flag_wait<0>(L + F_XN, 2);
    const u32 tag = flag_ld(L + F_TAG);
    if (!(lane & 8)) st_unit(wsr, (kg * 8 + (lane & 7)) * 16, qxb + rbk * 2048 + cb * 512, t2[0], t2[1], tag);
    STAMP(3);
    wait_until<1>(now() + QPD);
    wait_until<1>(t1 + P_QT);
    wait_until<1>(tf + P_QF);
    STAMP(7);
    u32x4 u[8];
#pragma nounroll
    for (u32 it = 0; it < SPIN_G; ++it) {
      sweep8(u, wsr, lane * 16, qxb);
      if (ok8(u, 0u, tag)) break;
    }
    STAMP(4);
#pragma unroll
    for (int h = 0; h < 2; ++h) {
      const float q0 = (asf(u[h].x) + asf(u[2 + h].x)) + (asf(u[4 + h].x) + asf(u[6 + h].x));
      const float q1 = (asf(u[h].z) + asf(u[2 + h].z)) + (asf(u[4 + h].z) + asf(u[6 + h].z));
      lds_st<u32>(L + L_Q + h * 256 + ((lane & 32) + (lane & 7) * 4 + ((lane >> 3) & 3)) * 4, pk2(q0, q1));
    }
    flag_st(L + F_Q, 1);
    STAMP(5);
    flag_wait<1>(L + F_P, 4);
    {
      const f32x4 m4 = lds_ld<f32x4>(L + L_ML + (lane & 1) * 32), l4 = lds_ld<f32x4>(L + L_ML + (lane & 1) * 32 + 16);
      const float M = vmax(vmax(m4[0], m4[1]), vmax(m4[2], m4[3]));
      float l = 0.f;
#pragma unroll
      for (int i = 0; i < 4; ++i) l = fmaf(__builtin_amdgcn_exp2f((m4[i] - M) * LOG2E), l4[i], l);
      if (lane < 8) st_unit(wsr, lane * 16, WS_ST + g * 2048 + s * 128, M, l, tag);
    }
    STAMP(6);
    if (b < 36) {
      const u32 hb2 = lds_ld<u32>(L + L_G + (lane & 3) * 4);
      const float b2f = lo16(lds_ld<u32>(L + L_G + 16));
      flag_wait<2>(L + F_PROG, 64);
      wait_until<0>(now() + P_RD);
      hint_wait<15, 0>(wsr, lane * 256, WS_C2 + cbu * 1024, hb2);
      flag_wait<0>(L + F_O, 1);
#pragma nounroll
      for (u32 it = 0; it < SPIN_G; ++it) {
        sweep8(u, wsr, lane * 16, WS_Y + b * 8192);
        if (ok8(u, 0u, tag)) break;
      }
      lds_st<float>(L + L_G + 32 + ((lane & 7) + (lane >> 5) * 8) * 4, gate_of(sum64(u), b2f));
      flag_st(L + F_G, 1);
    }
    STAMP_OUT();
    return;
  }

  if (wave == 2) {
    __builtin_amdgcn_s_setprio(3);
    asm volatile("s_waitcnt vmcnt(5)" : "+v"(xa), "+v"(xb)::"memory");
    float xv[9];
#pragma unroll
    for (int i = 0; i < 4; ++i) { xv[2 * i] = lo16(xa[i]); xv[2 * i + 1] = hi16(xa[i]); }
    xv[8] = lo16(xb);
    lds_st<u32x4>(L + L_X + lane * 16, xa);
    lds_st<u16>(L + L_X + 1024 + lane * 2, (u16)xb);
#pragma nounroll
    for (int pass = 0; pass < 2; ++pass) {
      asm volatile("" : "+s"(pass));
      ln9(xv, xv);
    const lptr dst = L + L_XN + pass * (L_H - L_XN);
      lds_st<u32x4>(dst + lane * 16, u32x4{pk2(xv[0], xv[1]), pk2(xv[2], xv[3]), pk2(xv[4], xv[5]), pk2(xv[6], xv[7])});
      lds_st<__bf16>(dst + 1024 + lane * 2, (__bf16)xv[8]);
      flag_st(L + F_XN, 1 + pass);
      if (pass == 0) {
        asm volatile("s_waitcnt vmcnt(4)" : "+v"(ep)::"memory");
        lds_st<u32>(L + F_TAG, __builtin_amdgcn_readlane(ep, 4) + 1u);
        lds_st<u32>(L + F_B1, __builtin_amdgcn_readlane(ep, 5));
        flag_st(L + F_XN, 2);
      }
    }
    STAMP(2);
    asm volatile("s_waitcnt vmcnt(0)" : "+v"(b2v), "+v"(par)::"memory");
    lds_st<u32x4>(L + L_PAR + lane * 16, par);
    if (lane < 4) lds_st<u32>(L + L_G + lane * 4, ep);
    if (lane == 0) lds_st<u32>(L + L_G + 16, b2v);
    flag_st(L + F_XN, 3);
    if (b < 36 && b == cbu * 9) {
      u64 sv;
      asm volatile("s_mov_b64 %0, exec\n\ts_mov_b64 exec, 15\n\tbuffer_atomic_swap %1, %2, %3, %4 offen offset:4\n\ts_mov_b64 exec, %0"
                   : "=&s"(sv) : "v"(t1), "v"(lane * 256), "s"(wsr), "s"(WS_C2 + cbu * 1024) : "memory");
    }
    __builtin_amdgcn_s_setprio(0);
  }

  flag_wait<2>(L + F_XN, 3);
  if (wave != 2) STAMP(1);
  const u32 tag = flag_ld(L + F_TAG);
  const int wv = wave - 4;
  float ya0 = 0.f, ya1 = 0.f;
  {
    u32 p1[9], pb[9], p2[9];
    gate_par(L, kg, p1, pb, p2);
    const lptr hp = L + L_H + (cb * 144 + (wave == 2 ? 112 : wv * 32) + n16) * 2;
#pragma nounroll
    for (int t = 1 - (wave >> 2); t < 2; ++t) {
      asm volatile("" : "+s"(t));
      ya0 = ya1;
      ya1 = gate_tile(lo16(lds_ld<u16>(hp + t * 32)), p1, pb, p2);
    }
  }

  if (wave == 2) {
    const int uu = (b - cbu * 9) * 8 + (lane & 7), hi = lane >> 5;
    const int col = b < 36 ? cbu * 144 + (uu < 64 ? (uu >> 4) * 32 + (uu & 15) + hi * 16 : 128 + (uu - 64) + hi * 8) : 0;
    const float xc = lo16(lds_ld<u16>(L + L_X + col * 2));
    const float yn = dpp<ROR8>(ya1);
    flag_wait<4>(L + F_PROG, 64);
    if (lane < 8) st_unit(wsr, lane * 16, WS_Y + (cb * 9 + 8) * 8192 + pi * 128, ya1, yn, tag);
    if (b < 36) {
      __builtin_amdgcn_s_setprio(3);
      u32 ov = (lane & 24) ? 0xfffffff0u : col * 2, ev = b == 0 && lane < 8 ? lane * 16 : 0xfffffff0u, sv16 = lane * 16;
      const u32 h2b = WS_H2 + b * 8192;
      asm volatile("" : "+v"(ov), "+v"(ev), "+v"(sv16) : "v"(xc), "s"(h2b), "s"(h2b + 4096));
      flag_wait<1>(L + F_O, 1);
      STAMP(4);
      const u32 tm = hint_wait_time(wsr, lane * 256, WS_C2 + cbu * 1024, ep);
      STAMP(3);
      wait_until<0>(tm - t1 < 4096u ? tm : t1);
      STAMP(7);
#pragma nounroll
      for (u32 it = 0; it < SPIN_G; ++it) {
        u32x4 u[8];
        asm volatile("buffer_load_dwordx4 %0, %8, %9, %10 offen sc1\n\tbuffer_load_dwordx4 %1, %8, %9, %10 offen offset:1024 sc1\n\t"
                     "buffer_load_dwordx4 %2, %8, %9, %10 offen offset:2048 sc1\n\tbuffer_load_dwordx4 %3, %8, %9, %10 offen offset:3072 sc1\n\t"
                     "buffer_load_dwordx4 %4, %8, %9, %11 offen sc1\n\tbuffer_load_dwordx4 %5, %8, %9, %11 offen offset:1024 sc1\n\t"
                     "buffer_load_dwordx4 %6, %8, %9, %11 offen offset:2048 sc1\n\tbuffer_load_dwordx4 %7, %8, %9, %11 offen offset:3072 sc1"
                     : "=&v"(u[0]), "=&v"(u[1]), "=&v"(u[2]), "=&v"(u[3]), "=&v"(u[4]), "=&v"(u[5]), "=&v"(u[6]), "=&v"(u[7])
                     : "v"(sv16), "s"(wsr), "s"(h2b), "s"(h2b + 4096) : "memory");
        flag_wait<0>(L + F_G, 1);
        float gate = lds_ld<float>(L + L_G + 32 + ((lane & 7) + hi * 8) * 4);
        asm volatile("" : "+v"(gate));
        STAMP(5);
        float a0 = 0.f, a1 = 0.f;
        u32 d = 0, e = 0;
#define H2STEP(i) asm volatile("s_waitcnt vmcnt(%1)" : "+v"(u[i]) : "i"(7 - i) : "memory"); a0 += asf(u[i].x); a1 += asf(u[i].z); \
        d = __builtin_amdgcn_bitop3_b32(u[i].y, tag, d, 0xBE); e = __builtin_amdgcn_bitop3_b32(u[i].w, tag, e, 0xBE); SBAR();
        H2STEP(0) H2STEP(1) H2STEP(2) H2STEP(3) H2STEP(4) H2STEP(5) H2STEP(6) H2STEP(7)
        const u64 bad = __builtin_amdgcn_ballot_w64((d | e) != 0);
        const bool ok = bad == 0;
        float tot = swap32_sum(a0, a1);
        tot = swap16_sum(tot, tot);
        tot = rb(tot + dpp<ROR8>(tot));
        const u16 res = __builtin_bit_cast(u16, (__bf16)(xc + rb(tot * gate)));
        __builtin_amdgcn_raw_buffer_store_b16(res, __builtin_amdgcn_make_buffer_rsrc((void*)B[1], 0, ok ? 1152 : 0, 0x00020000), ov, 0, OUTAUX);
        st_unit(__builtin_amdgcn_make_buffer_rsrc(ws, 0, ok ? 128 : 0, 0x00020000), ev, WS_EPOCH, asf(tag), asf(tag), tag);
        if (ok) break;
        asm volatile("s_waitcnt vmcnt(0)" ::: "memory");
      }
      STAMP(6);
    }
    STAMP_OUT();
    return;
  }

  {
    STAMP(2);
    __builtin_amdgcn_s_setprio(2);
    flag_wait<1>(L + F_KP, 16 * wv + 16);
    u32x4 kf[4][4];
#pragma unroll
    for (int t = 0; t < 4; ++t) {
      const lptr ka = L + L_K + wv * 16384 + n16 * 256 + (((4 * t + kg) ^ n16) << 4);
#pragma unroll
      for (int T = 0; T < 4; ++T) kf[T][t] = lds_ld<u32x4>(ka + T * 4096);
    }
    const int q4 = n16 >> 2, p4 = lane & 3;
    lptr va[2][2];
#pragma unroll
    for (int t = 0; t < 2; ++t)
#pragma unroll
      for (int nt = 0; nt < 2; ++nt)
        va[nt][t] = L + L_V + (8 * kg + 4 * t + q4) * 256 + (((2 * (2 * wv + nt) + (p4 >> 1)) ^ ((q4 << 2) | ((2 * kg + t) & 3))) << 4) + (p4 & 1) * 8;
    asm volatile("" ::"v"(va[0][0]), "v"(va[0][1]), "v"(va[1][0]), "v"(va[1][1]));
    SBAR();
    STAMP(3);
    u32x4 qf[4];
    q_wait(qf, L + L_Q + (n16 & 1) * 256 + kg * 16, L + F_Q);
    f32x4 sc[4];
#pragma unroll
    for (int T = 0; T < 4; ++T) sc[T] = mfma(qf[0], kf[T][0], f32x4{0.f, 0.f, 0.f, 0.f});
#pragma unroll
    for (int t = 1; t < 4; ++t)
#pragma unroll
      for (int T = 0; T < 4; ++T) sc[T] = mfma(qf[t], kf[T][t], sc[T]);
    const float sv0 = rb(rb(sel4(kg, sc[0][0], sc[1][0], sc[2][0], sc[3][0])) * 0.08838834764831845f);
    const float sv1 = rb(rb(sel4(kg, sc[0][1], sc[1][1], sc[2][1], sc[3][1])) * 0.08838834764831845f);
    float mx0 = sv0, mx1 = sv1;
    row_max2(mx0, mx1);
    mx0 = xrow_max(mx0); mx1 = xrow_max(mx1);
    const float ev0 = __builtin_amdgcn_exp2f((sv0 - mx0) * LOG2E), ev1 = __builtin_amdgcn_exp2f((sv1 - mx1) * LOG2E);
    lds_st<__bf16>(L + L_P + wv * 128 + lane * 2, (__bf16)ev0);
    lds_st<__bf16>(L + L_P + 512 + wv * 128 + lane * 2, (__bf16)ev1);
    float l01 = swap32_sum(ev0, ev1);
    l01 = row_sum(swap16_sum(l01, l01));
    if ((lane & 31) == 0) {
      lds_st<float>(L + L_ML + lane + wv * 4, lane ? mx1 : mx0);
      lds_st<float>(L + L_ML + lane + 16 + wv * 4, l01);
    }
    flag_add(LB + F_P);
    STAMP(4);
    const u32 h1s = WS_H1 + g * 32768 + wv * 8192 + s * 512;
    flag_wait<0>(L + F_P, 4);
    const f32x4 m40 = lds_ld<f32x4>(L + L_ML), m41 = lds_ld<f32x4>(L + L_ML + 32);
    u32x4 pf[8];
#pragma unroll
    for (int ks = 0; ks < 8; ++ks) pf[ks] = lds_ld<u32x4>(L + L_P + (n16 & 1) * 512 + kg * 16 + ks * 64);
    SBAR();
    float al[2][4];
    {
      const float M0 = vmax(vmax(m40[0], m40[1]), vmax(m40[2], m40[3])), M1 = vmax(vmax(m41[0], m41[1]), vmax(m41[2], m41[3]));
#pragma unroll
      for (int i = 0; i < 4; ++i) {
        al[0][i] = __builtin_amdgcn_exp2f((m40[i] - M0) * LOG2E); al[1][i] = __builtin_amdgcn_exp2f((m41[i] - M1) * LOG2E);
        asm volatile("" : "+v"(al[0][i]), "+v"(al[1][i]));
      }
      SBAR();
    }
    flag_wait<0>(L + F_PROG, 32);
    u32x4 b0[4], b1[4];
#pragma unroll
    for (int ks = 0; ks < 4; ++ks) {
      b0[ks] = cat(trd(va[0][0] + ks * 8192), trd(va[0][1] + ks * 8192));
      b1[ks] = cat(trd(va[1][0] + ks * 8192), trd(va[1][1] + ks * 8192));
    }
    SBAR();
    STAMP(5);
    const f32x4 z4 = {0.f, 0.f, 0.f, 0.f};
    float r0, r1;
    {
      f32x4 c0[3], c1[3];
#pragma unroll
      for (int sw = 0; sw < 2; ++sw) {
        c0[sw] = mfma(pf[2 * sw], b0[2 * sw], z4); c1[sw] = mfma(pf[2 * sw], b1[2 * sw], z4);
        c0[sw] = mfma(pf[2 * sw + 1], b0[2 * sw + 1], c0[sw]); c1[sw] = mfma(pf[2 * sw + 1], b1[2 * sw + 1], c1[sw]);
      }
      SBAR();
      flag_wait<0>(L + F_PROG, 48);
#pragma unroll
      for (int ks = 0; ks < 2; ++ks) {
        b0[ks] = cat(trd(va[0][0] + (4 + ks) * 8192), trd(va[0][1] + (4 + ks) * 8192));
        b1[ks] = cat(trd(va[1][0] + (4 + ks) * 8192), trd(va[1][1] + (4 + ks) * 8192));
      }
      SBAR();
      r0 = fmaf(al[0][1], kg ? c1[1][0] : c0[1][0], al[0][0] * (kg ? c1[0][0] : c0[0][0]));
      r1 = fmaf(al[1][1], kg ? c1[1][1] : c0[1][1], al[1][0] * (kg ? c1[0][1] : c0[0][1]));
      c0[2] = mfma(pf[4], b0[0], z4); c1[2] = mfma(pf[4], b1[0], z4);
      c0[2] = mfma(pf[5], b0[1], c0[2]); c1[2] = mfma(pf[5], b1[1], c1[2]);
      r0 = fmaf(al[0][2], kg ? c1[2][0] : c0[2][0], r0);
      r1 = fmaf(al[1][2], kg ? c1[2][1] : c0[2][1], r1);
    }
    u32 h1v = lane * 16, yv = (lane >> 3) * 8192 + (lane & 7) * 16;
    const u32 ys = WS_Y + (cb * 9 + wv * 2) * 8192 + pi * 128;
    asm volatile("" : "+v"(r0), "+v"(r1), "+v"(al[0][3]), "+v"(al[1][3]), "+v"(h1v), "+v"(yv) : "s"(h1s), "s"(ys));
    STAMP(6);
    {
      u32x4 x0, x1;
      v_step<6>(x0, x1, va, L + F_PROG);
      f32x4 c0 = mfma(pf[6], x0, z4), c1 = mfma(pf[6], x1, z4);
      SBAR();
      flag_add(LB + F_PV);
      v_step<7>(x0, x1, va, L + F_PROG);
      c0 = mfma(pf[7], x0, c0);
      c1 = mfma(pf[7], x1, c1);
      r0 = fmaf(al[0][3], kg ? c1[0] : c0[0], r0);
      r1 = fmaf(al[1][3], kg ? c1[1] : c0[1], r1);
    }
    if (lane < 32) st_unit(wsr, h1v, h1s, r0, r1, tag);
    SBAR();
    if (lane < 16) st_unit(wsr, yv, ys, ya0, ya1, tag);
    STAMP(7);
    STAMP_OUT();
  }
}

extern "C" void kernel_launch(void* const* d_in, const int* in_sizes, int n_in, void* d_out, int out_size, void* d_ws, size_t ws_size,
                              hipStream_t stream) {
  const char *kp = (const char*)d_in[1], *vp = (const char*)d_in[2], *qp = (const char*)d_in[3], *op = (const char*)d_in[4];
  const size_t w0 = (size_t)(SK - WIN) * 256, w1 = (size_t)SK * 256 - 64;
  Touch t = {{kp + w0, kp + w1, vp + w0, vp + w1, op, op + 255 * 1152, qp, qp + (2 << 20), qp + (4 << 20), d_in[0], d_in[6], d_in[7], d_in[8],
                    d_out, (const char*)d_ws + 2048, (const char*)d_ws + WS_END - 64}};
  hipLaunchKernelGGL(fused_decode, dim3(256), dim3(512), 0, stream, (const u16*)d_in[3], (const u16*)d_in[0], (const u16*)d_in[6],
                     (const u16*)d_in[7], (const u16*)d_in[8], (const u16*)d_in[1], (const u16*)d_in[2], (const u16*)d_in[4],
                     (const u16*)d_in[9], (u16*)d_out, (char*)d_ws, t);
}
```

```cpp
#define CODE_BYTES 15744
#define P_ROT 21
#define P_PACE 3533
#define P_KV 12
#include <hip/hip_runtime.h>
#include <stdint.h>

#define LAS __attribute__((address_space(3)))
typedef unsigned u32;
typedef unsigned long long u64;
typedef unsigned short u16;
typedef u32 u32x4 __attribute__((ext_vector_type(4)));
typedef u32 u32x2 __attribute__((ext_vector_type(2)));
typedef float f32x4 __attribute__((ext_vector_type(4)));
typedef float f32x2 __attribute__((ext_vector_type(2)));
typedef __bf16 bf16x8 __attribute__((ext_vector_type(8)));
typedef __bf16 bf16x2 __attribute__((ext_vector_type(2)));
typedef short s16x4 __attribute__((ext_vector_type(4)));
typedef LAS char* lptr;
#define GAS __attribute__((address_space(1)))
typedef u64 u64x8 __attribute__((ext_vector_type(8)));
typedef u64 u64x2 __attribute__((ext_vector_type(2)));

#define WOPOL " nt"
#define OUTAUX 0
#define NUMA 1
constexpr int WCAP = 44;
constexpr int QPD = 50;
#define P_BURST 3
#define P_QT 310
#define P_QF 110
#define P_WOA 40
#define P_WOB 48
#define P_H1D 8
#define P_H2D 42
#define P_MODE 30
#define P_BD 758658373
#define P_PT 6
#define P_WOA2 60
#define P_TR 180
#define P_BETA 10
#define P_QB 8
#define P_RD 30
#define DBG_ARG
#define DBG_PASS
#define DUE(n) ((u32)(((n) - P_BURST) * P_PACE) >> 10)
constexpr int SK = 65536, WIN = 4096;
constexpr u32 WS_EPOCH = 0;
constexpr u32 WS_C2 = 4096;
constexpr u32 WS_T0 = 16384;
constexpr u32 WS_G = 32768, G_SIZE = 81920;
constexpr u32 G_ST = 0;
constexpr u32 G_C1 = 2048;
constexpr u32 G_PR = 4096;
constexpr u32 G_H1 = 16384;
constexpr u32 WS_H2 = WS_G + 16 * G_SIZE;
constexpr u32 WS_QX = WS_H2 + 36 * 16384;
constexpr u32 WS_Y = WS_QX + 16 * 8192;
constexpr u32 WS_END = WS_Y + 288 * 1024;
constexpr u32 L_P = 0, L_Q = 1024, L_XN = 1536, L_H = 2688, L_X = 3840, L_G = 4992, L_Z = 5120, L_ML = 5248, L_F = 5312, L_PAR = 5440,
              L_WO = 6400 + 256, L_K = 24832 + 256, L_V = L_K + 65536, L_END = L_V + 65536;
constexpr u32 F_PROG = L_F, F_XN = L_F + 4, F_PI = L_F + 8, F_Q = L_F + 12, F_P = L_F + 16, F_PV = L_F + 20, F_O = L_F + 24, F_TAG = L_F + 28, F_KP = L_F + 32, F_G = L_F + 36, F_SH = L_F + 40, F_V = L_F + 44, F_B1 = L_F + 48, F_A = L_F + 56, F_W = L_F + 60, F_T1 = L_F + 64;
constexpr u32 L_W = L_Q, L_O = L_Q + 128;
constexpr u32 SPIN_G = 1u << 16, SPIN_L = 1u << 22;
constexpr float LOG2E = 1.4426950408889634f;

__device__ __forceinline__ float asf(u32 x) { return __builtin_bit_cast(float, x); }
__device__ __forceinline__ u32 asu(float x) { return __builtin_bit_cast(u32, x); }
__device__ __forceinline__ float lo16(u32 x) { return asf(x << 16); }
__device__ __forceinline__ float hi16(u32 x) { return asf(x & 0xffff0000u); }
__device__ __forceinline__ u32 pk2(float a, float b) { bf16x2 v = {(__bf16)a, (__bf16)b}; return __builtin_bit_cast(u32, v); }
__device__ __forceinline__ float rb(float a) { return (float)(__bf16)a; }
__device__ __forceinline__ float dot2(u32 a, u32 b, float c) {
  return __builtin_amdgcn_fdot2_f32_bf16(__builtin_bit_cast(bf16x2, a), __builtin_bit_cast(bf16x2, b), c, false);
}
template <int CTRL> __device__ __forceinline__ float dpp(float x) {
  return asf(__builtin_amdgcn_mov_dpp((int)asu(x), CTRL, 0xf, 0xf, true));
}
constexpr int XOR1 = 0xB1, XOR2 = 0x4E, HMIR = 0x141, MIR = 0x140, ROR8 = 0x128;
__device__ __forceinline__ float swap16_sum(float a, float b) {
  auto s = __builtin_amdgcn_permlane16_swap(asu(a), asu(b), false, false);
  return asf(s[0]) + asf(s[1]);
}
__device__ __forceinline__ float swap32_sum(float a, float b) {
  auto s = __builtin_amdgcn_permlane32_swap(asu(a), asu(b), false, false);
  return asf(s[0]) + asf(s[1]);
}
__device__ __forceinline__ float vmax(float a, float b) { float r; asm("v_max_f32 %0, %1, %2" : "=v"(r) : "v"(a), "v"(b)); return r; }
__device__ __forceinline__ float row_sum(float x) {
  x += dpp<XOR1>(x); x += dpp<XOR2>(x); x += dpp<HMIR>(x); x += dpp<MIR>(x); return x;
}
__device__ __forceinline__ void row_max2(float& a, float& b) {
  asm("s_nop 1\n\t"
      "v_max_f32_dpp %0, %0, %0 quad_perm:[1,0,3,2] row_mask:0xf bank_mask:0xf\n\t"
      "v_max_f32_dpp %1, %1, %1 quad_perm:[1,0,3,2] row_mask:0xf bank_mask:0xf\n\ts_nop 0\n\t"
      "v_max_f32_dpp %0, %0, %0 quad_perm:[2,3,0,1] row_mask:0xf bank_mask:0xf\n\t"
      "v_max_f32_dpp %1, %1, %1 quad_perm:[2,3,0,1] row_mask:0xf bank_mask:0xf\n\ts_nop 0\n\t"
      "v_max_f32_dpp %0, %0, %0 row_half_mirror row_mask:0xf bank_mask:0xf\n\t"
      "v_max_f32_dpp %1, %1, %1 row_half_mirror row_mask:0xf bank_mask:0xf\n\ts_nop 0\n\t"
      "v_max_f32_dpp %0, %0, %0 row_mirror row_mask:0xf bank_mask:0xf\n\t"
      "v_max_f32_dpp %1, %1, %1 row_mirror row_mask:0xf bank_mask:0xf\n\ts_nop 1"
      : "+v"(a), "+v"(b));
}
__device__ __forceinline__ float half_max(float a) {
  asm("s_nop 1\n\tv_max_f32_dpp %0, %0, %0 quad_perm:[1,0,3,2] row_mask:0xf bank_mask:0xf\n\ts_nop 1\n\t"
      "v_max_f32_dpp %0, %0, %0 quad_perm:[2,3,0,1] row_mask:0xf bank_mask:0xf\n\ts_nop 1\n\t"
      "v_max_f32_dpp %0, %0, %0 row_half_mirror row_mask:0xf bank_mask:0xf\n\ts_nop 1\n\t"
      "v_max_f32_dpp %0, %0, %0 row_mirror row_mask:0xf bank_mask:0xf\n\ts_nop 1" : "+v"(a));
  auto s = __builtin_amdgcn_permlane16_swap(asu(a), asu(a), false, false);
  return vmax(asf(s[0]), asf(s[1]));
}
__device__ __forceinline__ float xrow_max(float a) {
  auto s = __builtin_amdgcn_permlane16_swap(asu(a), asu(a), false, false);
  a = vmax(asf(s[0]), asf(s[1]));
  asm("s_nop 1" : "+v"(a));
  auto t = __builtin_amdgcn_permlane32_swap(asu(a), asu(a), false, false);
  return vmax(asf(t[0]), asf(t[1]));
}
__device__ __forceinline__ float wave_sum(float x) { x = row_sum(x); x = swap16_sum(x, x); return swap32_sum(x, x); }
__device__ __forceinline__ f32x4 mfma(u32x4 a, u32x4 b, f32x4 c) {
  return __builtin_amdgcn_mfma_f32_16x16x32_bf16(__builtin_bit_cast(bf16x8, a), __builtin_bit_cast(bf16x8, b), c, 0, 0, 0);
}
__device__ __forceinline__ u32x4 cat(s16x4 a, s16x4 b) { return __builtin_bit_cast(u32x4, __builtin_shufflevector(a, b, 0, 1, 2, 3, 4, 5, 6, 7)); }
__device__ __forceinline__ s16x4 trd(lptr p) { return __builtin_amdgcn_ds_read_tr16_b64_v4i16((LAS s16x4*)p); }

#define CBAR() asm volatile("" ::: "memory")
#define SBAR() __builtin_amdgcn_sched_barrier(0)
template <typename T> __device__ __forceinline__ T lds_ld(lptr p) { return *(const LAS T*)p; }
template <typename T> __device__ __forceinline__ void lds_st(lptr p, T v) { *(LAS T*)p = v; }
__device__ __forceinline__ u32 flag_ld(lptr p) { return __builtin_amdgcn_readfirstlane(*(volatile LAS u32*)p); }
__device__ __forceinline__ void flag_st(lptr p, u32 v) { CBAR(); *(volatile LAS u32*)p = v; CBAR(); }
__device__ __forceinline__ void flag_add(u32 a) {
  u64 sv;
  asm volatile("s_mov_b64 %0, exec\n\ts_mov_b64 exec, 1\n\tds_add_u32 %1, %2\n\ts_mov_b64 exec, %0" : "=&s"(sv) : "v"(a), "v"(1u) : "memory");
}
template <int SLEEP> __device__ __forceinline__ void flag_wait(lptr p, u32 v) {
  u32 t, n;
  asm volatile("s_mov_b32 %1, %5\n"
               "1:\n\tds_read_b32 %0, %2\n\ts_waitcnt lgkmcnt(0)\n\tv_cmp_le_u32 vcc, %3, %0\n\ts_cbranch_vccnz 2f\n\t"
               "s_sleep %4\n\ts_sub_u32 %1, %1, 1\n\ts_cbranch_scc0 1b\n2:"
               : "=&v"(t), "=&s"(n) : "v"(p), "s"(v), "i"(SLEEP), "i"(SPIN_L) : "vcc", "scc", "memory");
}
__device__ __forceinline__ u32 now() {
  u64 t;
  asm volatile("s_memrealtime %0\n\ts_waitcnt lgkmcnt(0)" : "=s"(t)::"memory");
  return (u32)t;
}
template <int SLEEP> __device__ __forceinline__ void wait_until(u32 due) {
  u32 n;
  asm volatile("s_movk_i32 %0, 0x1000\n"
               "1:\n\ts_memrealtime s[60:61]\n\ts_waitcnt lgkmcnt(0)\n\ts_sub_u32 s60, s60, %1\n\ts_cmp_ge_i32 s60, 0\n\ts_cbranch_scc1 2f\n\t"
               "s_sleep %2\n\ts_sub_u32 %0, %0, 1\n\ts_cbranch_scc0 1b\n2:"
               : "=&s"(n) : "s"(due), "i"(SLEEP) : "s60", "s61", "scc", "memory");
}

typedef __amdgpu_buffer_rsrc_t rsrc_t;
__device__ __forceinline__ u32x4 ld_unit(rsrc_t r, u32 voff, u32 soff) { return __builtin_amdgcn_raw_buffer_load_b128(r, voff, soff, 16); }
__device__ __forceinline__ void st_unit(rsrc_t r, u32 voff, u32 soff, float a, float b, u32 tag) {
  u32x4 v = {asu(a), tag, asu(b), tag};
  __builtin_amdgcn_raw_buffer_store_b128(v, r, voff, soff, 16);
}
__device__ __forceinline__ void sweep8(u32x4 (&u)[8], rsrc_t r, u32 voff, u32 soff) {
  CBAR();
#pragma unroll
  for (int i = 0; i < 8; ++i) u[i] = ld_unit(r, voff + (i & 3) * 1024, soff + (i >> 2) * 4096);
}
__device__ __forceinline__ void sweep8_async(u32x4 (&u)[8], rsrc_t r, u32 voff, u32 soff) {
  asm volatile("buffer_load_dwordx4 %0, %8, %9, %10 offen sc1\n\tbuffer_load_dwordx4 %1, %8, %9, %10 offen offset:1024 sc1\n\t"
               "buffer_load_dwordx4 %2, %8, %9, %10 offen offset:2048 sc1\n\tbuffer_load_dwordx4 %3, %8, %9, %10 offen offset:3072 sc1\n\t"
               "buffer_load_dwordx4 %4, %8, %9, %11 offen sc1\n\tbuffer_load_dwordx4 %5, %8, %9, %11 offen offset:1024 sc1\n\t"
               "buffer_load_dwordx4 %6, %8, %9, %11 offen offset:2048 sc1\n\tbuffer_load_dwordx4 %7, %8, %9, %11 offen offset:3072 sc1"
               : "=&v"(u[0]), "=&v"(u[1]), "=&v"(u[2]), "=&v"(u[3]), "=&v"(u[4]), "=&v"(u[5]), "=&v"(u[6]), "=&v"(u[7])
               : "v"(voff), "s"(r), "s"(soff), "s"(soff + 4096) : "memory");
}
__device__ __forceinline__ u32 bad_tags(u32 d, u32x4 u, u32 tag) {
  d = __builtin_amdgcn_bitop3_b32(u.y, tag, d, 0xBE);
  return __builtin_amdgcn_bitop3_b32(u.w, tag, d, 0xBE);
}
__device__ __forceinline__ bool ok8(const u32x4 (&u)[8], u32 d, u32 tag) {
  u32 e = 0;
#pragma unroll
  for (int i = 0; i < 8; ++i) {
    d = __builtin_amdgcn_bitop3_b32(u[i].y, tag, d, 0xBE);
    e = __builtin_amdgcn_bitop3_b32(u[i].w, tag, e, 0xBE);
  }
  return __builtin_amdgcn_ballot_w64((d | e) != 0) == 0;
}
template <int MASK> __device__ __forceinline__ void hint_add(rsrc_t r, u32 voff, u32 val) {
  u64 sv;
  asm volatile("s_mov_b64 %0, exec\n\ts_mov_b64 exec, %4\n\tbuffer_atomic_add %1, %2, %3, 0 offen\n\ts_mov_b64 exec, %0"
               : "=&s"(sv) : "v"(val), "v"(voff), "s"(r), "i"(MASK) : "memory");
}
template <int MASK, int SHIFT> __device__ __forceinline__ void hint_wait(rsrc_t r, u32 voff, u32 soff, u32 base) {
  u64 sv;
  u32 t, n;
  asm volatile("s_mov_b64 %0, exec\n\ts_mov_b64 exec, %7\n\ts_mov_b32 %2, 23\n"
               "1:\n\tv_mov_b32 %1, 0\n\tbuffer_atomic_add %1, %3, %4, %5 offen sc0\n\ts_waitcnt vmcnt(0)\n\t"
               "v_sub_u32 %1, %1, %6\n\tv_bfe_u32 %1, %1, %8, 16\n\tv_cmp_gt_u32 vcc, 16, %1\n\ts_cbranch_vccz 2f\n\ts_sub_u32 %2, %2, 1\n\ts_cbranch_scc0 1b\n"
               "2:\n\ts_mov_b64 exec, %0"
               : "=&s"(sv), "=&v"(t), "=&s"(n) : "v"(voff), "s"(r), "s"(soff), "v"(base), "i"(MASK), "i"(SHIFT) : "vcc", "scc", "memory");
}

__device__ __forceinline__ u32 hint_wait_time(rsrc_t r, u32 voff, u32 soff, u32 base) {
  u64 sv;
  u32 t, n, tm;
  asm volatile("s_mov_b64 %0, exec\n\ts_mov_b64 exec, 15\n\ts_mov_b32 %2, 23\n"
               "1:\n\tv_mov_b64 v[200:201], 0\n\tbuffer_atomic_add_x2 v[200:201], %4, %5, %6 offen sc0\n\ts_waitcnt vmcnt(0)\n\t"
               "v_sub_u32 %1, v200, %7\n\tv_lshrrev_b32 %1, 16, %1\n\tv_cmp_gt_u32 vcc, 16, %1\n\ts_cbranch_vccz 2f\n\ts_sub_u32 %2, %2, 1\n\ts_cbranch_scc0 1b\n"
               "2:\n\tv_max_u32_dpp v201, v201, v201 quad_perm:[1,0,3,2] row_mask:0xf bank_mask:0xf\n\ts_nop 1\n\t"
               "v_max_u32_dpp v201, v201, v201 quad_perm:[2,3,0,1] row_mask:0xf bank_mask:0xf\n\ts_nop 0\n\t"
               "v_readfirstlane_b32 %3, v201\n\ts_mov_b64 exec, %0"
               : "=&s"(sv), "=&v"(t), "=&s"(n), "=&s"(tm) : "v"(voff), "s"(r), "s"(soff), "v"(base) : "v200", "v201", "vcc", "scc", "memory");
  return tm;
}

#define STAMP(i)
#define STAMP0()
#define STAMP_DECL
#define STAMP_OUT() asm volatile("s_endpgm" ::: "memory")

__device__ __forceinline__ void dma1(u32 ldsaddr, const char* base, u32 o0) {
  asm volatile("s_mov_b32 m0, %0\n\ts_nop 0\n\tglobal_load_lds_dwordx4 %1, %2" WOPOL ::"s"(ldsaddr), "v"(o0), "s"(base) : "memory");
}
template <int N> __device__ __forceinline__ void prog_pub(u32 fa, int landed) {
  asm volatile("s_waitcnt vmcnt(%2)\n\tds_write_b32 %0, %1" ::"v"(fa), "v"(landed), "i"(N) : "memory");
}
template <int T, int STEP> __device__ __forceinline__ void drain(u32 fa) {
  if constexpr (T <= 64) {
    if constexpr (64 - T < WCAP) prog_pub<64 - T>(fa, T);
    drain<T + STEP, STEP>(fa);
  }
}
constexpr int RAMP0 = 30, KSTEP = 4 * 65536 / (142 - RAMP0);
__device__ __forceinline__ void load_tile(int FIRST, u32 lds, u64 base, int rot, u32 o0, u32 x1, u32 t1, u32 fa, lptr shp DBG_ARG) {
  const u32 o1 = o0 ^ x1, o2 = o0 ^ (2 * x1), o3 = o0 ^ (3 * x1);
  u32 duef = (FIRST - P_BURST) * P_PACE, prog = 4 - WCAP, t, pv;
  int kf = (FIRST - RAMP0) * (KSTEP / 4);
  asm volatile("s_mov_b64 s[66:67], %[base]\n\ts_mov_b32 s68, 0\n"
               "1:\n\tds_read_b32 %[t], %[shp]\n\ts_memrealtime s[60:61]\n"
               "2:\n\ts_max_i32 s62, %[kf], 0\n\ts_lshr_b32 s63, %[duef], 10\n\ts_add_u32 s63, s63, %[t1]\n\t"
               "s_and_b32 s64, %[rot], 14\n\ts_lshl_b32 s64, s64, 16\n\ts_and_b32 s65, %[rot], 1\n\ts_lshl_b32 s65, s65, 12\n\ts_or_b32 s64, s64, s65\n\t"
               "s_add_u32 s64, s64, s66\n\ts_addc_u32 s65, s67, 0\n\ts_mov_b32 m0, %[lds]\n\t"
               "s_waitcnt lgkmcnt(0)\n\tv_readfirstlane_b32 s61, %[t]\n\ts_sub_u32 s60, s60, s63\n\ts_mul_i32 s61, s61, s62\n\ts_ashr_i32 s61, s61, 16\n\t"
               "s_cmp_ge_i32 s60, s61\n\ts_cbranch_scc1 3f\n\t"
               "s_cmp_gt_i32 s60, -4096\n\ts_cbranch_scc1 1b\n"
               "3:\n\tglobal_load_lds_dwordx4 %[o0], s[64:65] nt\n\tglobal_load_lds_dwordx4 %[o1], s[64:65] offset:1024 nt\n\t"
               "ds_read_b32 %[t], %[shp]\n\ts_memrealtime s[60:61]\n\t"
               "global_load_lds_dwordx4 %[o2], s[64:65] offset:2048 nt\n\tglobal_load_lds_dwordx4 %[o3], s[64:65] offset:3072 nt\n\t"
               "s_waitcnt vmcnt(%[cap])\n\ts_max_i32 s68, s68, %[prog]\n\t"
               "s_getreg_b32 s62, hwreg(HW_REG_IB_STS, 22, 2)\n\ts_getreg_b32 s63, hwreg(HW_REG_IB_STS, 0, 4)\n\ts_lshl4_add_u32 s62, s62, s63\n\t"
               "s_cmp_gt_u32 s62, 24\n\ts_cbranch_scc1 4f\n\ts_waitcnt vmcnt(24)\n\ts_add_i32 s62, %[prog], %[cap]-24\n\ts_max_i32 s68, s68, s62\n"
               "4:\n\tv_mov_b32 %[pv], s68\n\tds_write_b32 %[fa], %[pv]\n\t"
               "s_add_u32 %[lds], %[lds], 0x1000\n\ts_add_u32 %[rot], %[rot], 1\n\ts_add_u32 %[duef], %[duef], %[dstep]\n\ts_add_i32 %[kf], %[kf], %[kstep]\n\t"
               "s_add_i32 %[prog], %[prog], 4\n\ts_cmp_lg_u32 %[prog], %[pend]\n\ts_cbranch_scc1 2b\n\ts_waitcnt lgkmcnt(0)"
               : [t] "=&v"(t), [pv] "=&v"(pv), [lds] "+s"(lds), [rot] "+s"(rot), [duef] "+s"(duef), [kf] "+s"(kf), [prog] "+s"(prog)
               : [base] "s"(base), [shp] "v"(shp), [t1] "s"(t1), [o0] "v"(o0), [o1] "v"(o1), [o2] "v"(o2), [o3] "v"(o3), [fa] "v"(fa),
                 [dstep] "s"(4 * P_PACE), [kstep] "i"(KSTEP), [cap] "i"(WCAP), [pend] "i"(68 - WCAP)
               : "s60", "s61", "s62", "s63", "s64", "s65", "s66", "s67", "s68", "scc", "memory");
}

__device__ __forceinline__ void ln9(const float (&v)[9], float (&o)[9]) {
  float s = ((v[0] + v[1]) + (v[2] + v[3])) + ((v[4] + v[5]) + (v[6] + v[7])) + v[8];
  const float mean = wave_sum(s) * (1.0f / 576.0f);
  float q = 0.f, q2 = 0.f;
#pragma unroll
  for (int i = 0; i < 9; ++i) { const float d = v[i] - mean; if (i & 1) q2 = fmaf(d, d, q2); else q = fmaf(d, d, q); }
  const float var = wave_sum(q + q2) * (1.0f / 576.0f);
  const float mb = rb(mean);
  const float den = rb(__builtin_amdgcn_sqrtf(rb(rb(var) + 1e-5f)));
  const float inv = __builtin_amdgcn_rcpf(den);
#pragma unroll
  for (int i = 0; i < 9; ++i) o[i] = rb(rb(v[i] - mb) * inv);
}
__device__ __forceinline__ float gelu1(float u) {
  constexpr float C0 = -2.0f * 0.7978845608028654f * LOG2E, C1 = C0 * 0.044715f;
  float t = u * u;
  asm("" : "+v"(t));
  float e = __builtin_amdgcn_exp2f(u * fmaf(t, C1, C0)) + 1.0f;
  asm("" : "+v"(e));
  float r = u * __builtin_amdgcn_rcpf(e);
  asm("" : "+v"(r));
  return r;
}
__device__ __forceinline__ float gate_tile(float hh, const u32 (&p1)[9], const u32 (&pb)[9], const u32 (&p2)[9]) {
  float acc = 0.f;
#pragma unroll
  for (int j = 0; j < 9; ++j) {
    float u0 = fmaf(hh, lo16(p1[j]), lo16(pb[j])), u1 = fmaf(hh, hi16(p1[j]), hi16(pb[j]));
    asm("" : "+v"(u0));
    asm("" : "+v"(u1));
    acc = dot2(pk2(gelu1(u0), gelu1(u1)), p2[j], acc);
  }
  acc = swap16_sum(acc, acc);
  return swap32_sum(acc, acc);
}
__device__ __forceinline__ void gate_par(lptr L, int kg, u32 (&p1)[9], u32 (&pb)[9], u32 (&p2)[9]) {
#pragma unroll
  for (int j = 0; j < 9; ++j) {
    const lptr pa = L + L_PAR + kg * 36 + j * 4;
    p1[j] = lds_ld<u32>(pa); pb[j] = lds_ld<u32>(pa + 256); p2[j] = lds_ld<u32>(pa + 512);
  }
}
template <int KS> __device__ __forceinline__ void v_early(u32x2 (&x)[4], const lptr (&va)[2][2], u32 seen) {
  asm volatile("s_cmp_lt_u32 %[seen], %[thr]\n\ts_cbranch_scc1 1f\n\t"
               "ds_read_b64_tr_b16 %0, %[a0] offset:%[o]+0\n\t"
               "ds_read_b64_tr_b16 %1, %[a1] offset:%[o]+0\n\t"
               "ds_read_b64_tr_b16 %2, %[a2] offset:%[o]+0\n\t"
               "ds_read_b64_tr_b16 %3, %[a3] offset:%[o]+0\n\t"
               "1:"
               : "=&v"(x[0]), "=&v"(x[1]), "=&v"(x[2]), "=&v"(x[3]) : [a0] "v"(va[0][0]), [a1] "v"(va[0][1]), [a2] "v"(va[1][0]), [a3] "v"(va[1][1]), [o] "i"(KS * 8192), [seen] "s"(seen), [thr] "i"(8 * (KS + 1)) : "scc", "memory");
}
template <int KS> __device__ __forceinline__ void v_late(u32x2 (&x)[4], const lptr (&va)[2][2], u32 seen, lptr prog) {
  u32 t, n;
  asm volatile("s_cmp_ge_u32 %[seen], %[thr]\n\ts_cbranch_scc1 2f\n\ts_mov_b32 %[n], %[spin]\n1:\n\tds_read_b32 %[t], %[p]\n\t"
               "ds_read_b64_tr_b16 %0, %[a0] offset:%[o]+0\n\t"
               "ds_read_b64_tr_b16 %1, %[a1] offset:%[o]+0\n\t"
               "ds_read_b64_tr_b16 %2, %[a2] offset:%[o]+0\n\t"
               "ds_read_b64_tr_b16 %3, %[a3] offset:%[o]+0\n\t"
               "s_waitcnt lgkmcnt(0)\n\tv_cmp_le_u32 vcc, %[thr], %[t]\n\ts_cbranch_vccnz 2f\n\ts_sub_u32 %[n], %[n], 1\n\ts_cbranch_scc0 1b\n2:"
               : "+v"(x[0]), "+v"(x[1]), "+v"(x[2]), "+v"(x[3]), [t] "=&v"(t), [n] "=&s"(n) : [a0] "v"(va[0][0]), [a1] "v"(va[0][1]), [a2] "v"(va[1][0]), [a3] "v"(va[1][1]), [o] "i"(KS * 8192), [seen] "s"(seen), [thr] "i"(8 * (KS + 1)), [p] "v"(prog), [spin] "i"(SPIN_L) : "vcc", "scc", "memory");
}
template <int KS> __device__ __forceinline__ void v_early(u32x2 (&x)[8], const lptr (&va)[2][2], u32 seen) {
  asm volatile("s_cmp_lt_u32 %[seen], %[thr]\n\ts_cbranch_scc1 1f\n\t"
               "ds_read_b64_tr_b16 %0, %[a0] offset:%[o]+0\n\t"
               "ds_read_b64_tr_b16 %1, %[a1] offset:%[o]+0\n\t"
               "ds_read_b64_tr_b16 %2, %[a2] offset:%[o]+0\n\t"
               "ds_read_b64_tr_b16 %3, %[a3] offset:%[o]+0\n\t"
               "ds_read_b64_tr_b16 %4, %[a0] offset:%[o]+8192\n\t"
               "ds_read_b64_tr_b16 %5, %[a1] offset:%[o]+8192\n\t"
               "ds_read_b64_tr_b16 %6, %[a2] offset:%[o]+8192\n\t"
               "ds_read_b64_tr_b16 %7, %[a3] offset:%[o]+8192\n\t"
               "1:"
               : "=&v"(x[0]), "=&v"(x[1]), "=&v"(x[2]), "=&v"(x[3]), "=&v"(x[4]), "=&v"(x[5]), "=&v"(x[6]), "=&v"(x[7]) : [a0] "v"(va[0][0]), [a1] "v"(va[0][1]), [a2] "v"(va[1][0]), [a3] "v"(va[1][1]), [o] "i"(KS * 8192), [seen] "s"(seen), [thr] "i"(8 * (KS + 2)) : "scc", "memory");
}
template <int KS> __device__ __forceinline__ void v_late(u32x2 (&x)[8], const lptr (&va)[2][2], u32 seen, lptr prog) {
  u32 t, n;
  asm volatile("s_cmp_ge_u32 %[seen], %[thr]\n\ts_cbranch_scc1 2f\n\ts_mov_b32 %[n], %[spin]\n1:\n\tds_read_b32 %[t], %[p]\n\t"
               "s_waitcnt lgkmcnt(0)\n\tv_cmp_le_u32 vcc, %[thr], %[t]\n\ts_cbranch_vccnz 3f\n\ts_sub_u32 %[n], %[n], 1\n\ts_cbranch_scc0 1b\n3:\n\t"
               "ds_read_b64_tr_b16 %0, %[a0] offset:%[o]+0\n\t"
               "ds_read_b64_tr_b16 %1, %[a1] offset:%[o]+0\n\t"
               "ds_read_b64_tr_b16 %2, %[a2] offset:%[o]+0\n\t"
               "ds_read_b64_tr_b16 %3, %[a3] offset:%[o]+0\n\t"
               "ds_read_b64_tr_b16 %4, %[a0] offset:%[o]+8192\n\t"
               "ds_read_b64_tr_b16 %5, %[a1] offset:%[o]+8192\n\t"
               "ds_read_b64_tr_b16 %6, %[a2] offset:%[o]+8192\n\t"
               "ds_read_b64_tr_b16 %7, %[a3] offset:%[o]+8192\n\t"
               "s_waitcnt lgkmcnt(0)\n2:"
               : "+v"(x[0]), "+v"(x[1]), "+v"(x[2]), "+v"(x[3]), "+v"(x[4]), "+v"(x[5]), "+v"(x[6]), "+v"(x[7]), [t] "=&v"(t), [n] "=&s"(n) : [a0] "v"(va[0][0]), [a1] "v"(va[0][1]), [a2] "v"(va[1][0]), [a3] "v"(va[1][1]), [o] "i"(KS * 8192), [seen] "s"(seen), [thr] "i"(8 * (KS + 2)), [p] "v"(prog), [spin] "i"(SPIN_L) : "vcc", "scc", "memory");
}
template <int KS> __device__ __forceinline__ void v_early(u32x2 (&x)[16], const lptr (&va)[2][2], u32 seen) {
  asm volatile("s_cmp_lt_u32 %[seen], %[thr]\n\ts_cbranch_scc1 1f\n\t"
               "ds_read_b64_tr_b16 %0, %[a0] offset:%[o]+0\n\t"
               "ds_read_b64_tr_b16 %1, %[a1] offset:%[o]+0\n\t"
               "ds_read_b64_tr_b16 %2, %[a2] offset:%[o]+0\n\t"
               "ds_read_b64_tr_b16 %3, %[a3] offset:%[o]+0\n\t"
               "ds_read_b64_tr_b16 %4, %[a0] offset:%[o]+8192\n\t"
               "ds_read_b64_tr_b16 %5, %[a1] offset:%[o]+8192\n\t"
               "ds_read_b64_tr_b16 %6, %[a2] offset:%[o]+8192\n\t"
               "ds_read_b64_tr_b16 %7, %[a3] offset:%[o]+8192\n\t"
               "ds_read_b64_tr_b16 %8, %[a0] offset:%[o]+16384\n\t"
               "ds_read_b64_tr_b16 %9, %[a1] offset:%[o]+16384\n\t"
               "ds_read_b64_tr_b16 %10, %[a2] offset:%[o]+16384\n\t"
               "ds_read_b64_tr_b16 %11, %[a3] offset:%[o]+16384\n\t"
               "ds_read_b64_tr_b16 %12, %[a0] offset:%[o]+24576\n\t"
               "ds_read_b64_tr_b16 %13, %[a1] offset:%[o]+24576\n\t"
               "ds_read_b64_tr_b16 %14, %[a2] offset:%[o]+24576\n\t"
               "ds_read_b64_tr_b16 %15, %[a3] offset:%[o]+24576\n\t"
               "1:"
               : "=&v"(x[0]), "=&v"(x[1]), "=&v"(x[2]), "=&v"(x[3]), "=&v"(x[4]), "=&v"(x[5]), "=&v"(x[6]), "=&v"(x[7]), "=&v"(x[8]), "=&v"(x[9]), "=&v"(x[10]), "=&v"(x[11]), "=&v"(x[12]), "=&v"(x[13]), "=&v"(x[14]), "=&v"(x[15]) : [a0] "v"(va[0][0]), [a1] "v"(va[0][1]), [a2] "v"(va[1][0]), [a3] "v"(va[1][1]), [o] "i"(KS * 8192), [seen] "s"(seen), [thr] "i"(8 * (KS + 4)) : "scc", "memory");
}
template <int KS> __device__ __forceinline__ void v_late(u32x2 (&x)[16], const lptr (&va)[2][2], u32 seen, lptr prog) {
  u32 t, n;
  asm volatile("s_cmp_ge_u32 %[seen], %[thr]\n\ts_cbranch_scc1 2f\n\ts_mov_b32 %[n], %[spin]\n1:\n\tds_read_b32 %[t], %[p]\n\t"
               "s_waitcnt lgkmcnt(0)\n\tv_cmp_le_u32 vcc, %[thr], %[t]\n\ts_cbranch_vccnz 3f\n\ts_sub_u32 %[n], %[n], 1\n\ts_cbranch_scc0 1b\n3:\n\t"
               "ds_read_b64_tr_b16 %0, %[a0] offset:%[o]+0\n\t"
               "ds_read_b64_tr_b16 %1, %[a1] offset:%[o]+0\n\t"
               "ds_read_b64_tr_b16 %2, %[a2] offset:%[o]+0\n\t"
               "ds_read_b64_tr_b16 %3, %[a3] offset:%[o]+0\n\t"
               "ds_read_b64_tr_b16 %4, %[a0] offset:%[o]+8192\n\t"
               "ds_read_b64_tr_b16 %5, %[a1] offset:%[o]+8192\n\t"
               "ds_read_b64_tr_b16 %6, %[a2] offset:%[o]+8192\n\t"
               "ds_read_b64_tr_b16 %7, %[a3] offset:%[o]+8192\n\t"
               "ds_read_b64_tr_b16 %8, %[a0] offset:%[o]+16384\n\t"
               "ds_read_b64_tr_b16 %9, %[a1] offset:%[o]+16384\n\t"
               "ds_read_b64_tr_b16 %10, %[a2] offset:%[o]+16384\n\t"
               "ds_read_b64_tr_b16 %11, %[a3] offset:%[o]+16384\n\t"
               "ds_read_b64_tr_b16 %12, %[a0] offset:%[o]+24576\n\t"
               "ds_read_b64_tr_b16 %13, %[a1] offset:%[o]+24576\n\t"
               "ds_read_b64_tr_b16 %14, %[a2] offset:%[o]+24576\n\t"
               "ds_read_b64_tr_b16 %15, %[a3] offset:%[o]+24576\n\t"
               "s_waitcnt lgkmcnt(0)\n2:"
               : "+v"(x[0]), "+v"(x[1]), "+v"(x[2]), "+v"(x[3]), "+v"(x[4]), "+v"(x[5]), "+v"(x[6]), "+v"(x[7]), "+v"(x[8]), "+v"(x[9]), "+v"(x[10]), "+v"(x[11]), "+v"(x[12]), "+v"(x[13]), "+v"(x[14]), "+v"(x[15]), [t] "=&v"(t), [n] "=&s"(n) : [a0] "v"(va[0][0]), [a1] "v"(va[0][1]), [a2] "v"(va[1][0]), [a3] "v"(va[1][1]), [o] "i"(KS * 8192), [seen] "s"(seen), [thr] "i"(8 * (KS + 4)), [p] "v"(prog), [spin] "i"(SPIN_L) : "vcc", "scc", "memory");
}
__device__ __forceinline__ u32x4 cat2(u32x2 a, u32x2 b) { return u32x4{a.x, a.y, b.x, b.y}; }
__device__ __forceinline__ void p_wait(u32x4 (&pf)[8], f32x4& m0, f32x4& m1, f32x2& own, const lptr (&pa)[4], lptr ml, lptr mo, lptr flag) {
  u32 t, n;
  asm volatile("s_mov_b32 %[n], %[spin]\n1:\n\tds_read_b32 %[t], %[f]\n\t"
               "ds_read_b128 %8, %[ml]\n\tds_read_b128 %9, %[ml] offset:32\n\tds_read2_b32 %10, %[mo] offset1:8\n\t"
               "ds_read_b128 %0, %[p0]\n\tds_read_b128 %1, %[p0] offset:64\n\tds_read_b128 %2, %[p1]\n\tds_read_b128 %3, %[p1] offset:64\n\t"
               "ds_read_b128 %4, %[p2]\n\tds_read_b128 %5, %[p2] offset:64\n\tds_read_b128 %6, %[p3]\n\tds_read_b128 %7, %[p3] offset:64\n\t"
               "s_waitcnt lgkmcnt(0)\n\tv_cmp_le_u32 vcc, 4, %[t]\n\ts_cbranch_vccnz 2f\n\ts_sub_u32 %[n], %[n], 1\n\ts_cbranch_scc0 1b\n2:"
               : "=&v"(pf[0]), "=&v"(pf[1]), "=&v"(pf[2]), "=&v"(pf[3]), "=&v"(pf[4]), "=&v"(pf[5]), "=&v"(pf[6]), "=&v"(pf[7]), "=&v"(m0), "=&v"(m1),
                 "=&v"(own), [t] "=&v"(t), [n] "=&s"(n)
               : [p0] "v"(pa[0]), [p1] "v"(pa[1]), [p2] "v"(pa[2]), [p3] "v"(pa[3]), [ml] "v"(ml), [mo] "v"(mo), [f] "v"(flag), [spin] "i"(SPIN_L)
               : "vcc", "scc", "memory");
}
__device__ __forceinline__ u32 q_wait(u32x4 (&q)[4], lptr qa, lptr flags) {
  u32 seen, n;
  asm volatile("s_mov_b32 %5, %8\n"
               "1:\n\tds_read2_b32 v[200:201], %7 offset1:3\n\tds_read_b128 %0, %6\n\tds_read_b128 %1, %6 offset:64\n\tds_read_b128 %2, %6 offset:128\n\tds_read_b128 %3, %6 offset:192\n\t"
               "s_waitcnt lgkmcnt(0)\n\tv_cmp_le_u32 vcc, 1, v201\n\ts_cbranch_vccnz 2f\n\ts_sub_u32 %5, %5, 1\n\ts_cbranch_scc0 1b\n2:\n\tv_readfirstlane_b32 %4, v200"
               : "=&v"(q[0]), "=&v"(q[1]), "=&v"(q[2]), "=&v"(q[3]), "=&s"(seen), "=&s"(n) : "v"(qa), "v"(flags), "i"(SPIN_L) : "v200", "v201", "vcc", "scc", "memory");
  return __builtin_amdgcn_readfirstlane(seen);
}
__device__ __forceinline__ float sum64(const u32x4 (&u)[8]) {
  const float a0 = ((asf(u[0].x) + asf(u[1].x)) + (asf(u[2].x) + asf(u[3].x))) + ((asf(u[4].x) + asf(u[5].x)) + (asf(u[6].x) + asf(u[7].x)));
  const float a1 = ((asf(u[0].z) + asf(u[1].z)) + (asf(u[2].z) + asf(u[3].z))) + ((asf(u[4].z) + asf(u[5].z)) + (asf(u[6].z) + asf(u[7].z)));
  float tot = swap32_sum(a0, a1);
  tot = swap16_sum(tot, tot);
  return rb(tot + dpp<ROR8>(tot));
}
__device__ __forceinline__ float gate_of(float s, float b2f) {
  const float y = rb(s + b2f);
  return rb(y * __builtin_amdgcn_rcpf(1.0f + __builtin_amdgcn_exp2f(-LOG2E * y)));
}
__device__ __forceinline__ float sel4(int kg, float a, float b, float c, float d) {
  const float lo = (kg & 1) ? b : a, hi = (kg & 1) ? d : c;
  return (kg & 2) ? hi : lo;
}

struct Touch {
  const void* p[16];
};

__global__ void __launch_bounds__(512, 2)
fused_decode(const u16* a_wq, const u16* a_x, const u16* a_w1, const u16* a_b1, const u16* a_w2, const u16* a_kc, const u16* a_vc,
             const u16* a_wo, const u16* a_b2, u16* a_out, char* a_ws, Touch a_touch) {
  __shared__ __attribute__((aligned(16))) char smem[L_END];
  const lptr L = (lptr)smem;
  const u32 LB = (u32)(uintptr_t)L;
  const int b = blockIdx.x, lane = threadIdx.x & 63, wave = __builtin_amdgcn_readfirstlane(threadIdx.x >> 6);
  u64x8 A;
  u64x2 B;
  u64 C;
  asm volatile("s_cmp_lg_u32 %4, 7\n\ts_cbranch_scc1 1f\n\tv_mov_b32 v200, 0\n\tv_and_b32 v201, 31, %5\n\tv_lshlrev_b32 v201, 2, v201\n\t"
               "ds_write_b32 v201, v200 offset:%6\n\ts_waitcnt lgkmcnt(0)\n1:\n\t"
               "s_load_dwordx16 %0, %3, 0x0\n\ts_load_dwordx4 %1, %3, 0x40\n\ts_load_dwordx2 %2, %3, 0x50"
               : "=&s"(A), "=&s"(B), "=&s"(C) : "s"(__builtin_amdgcn_kernarg_segment_ptr()), "s"(wave), "v"(threadIdx.x), "i"(L_F)
               : "v200", "v201", "scc", "memory");
  SBAR();
  STAMP_DECL;
  STAMP0();
  const int g = (b & 7) * 2 + (b >> 7), s = (b >> 3) & 15, rbk = s >> 2, cb = s & 3, pi = g * 4 + rbk;
  const int n16 = lane & 15, kg = lane >> 4, r8 = lane >> 3;
  u64 tp;
  u32 dump;
  asm volatile("s_cmp_lt_u32 %4, 4\n\ts_cbranch_scc1 2f\n\ts_cmp_lg_u32 %4, 4\n\ts_cbranch_scc1 1f\n\t"
               "s_load_dwordx2 %0, %2, %3 offset:0x58\n\ts_branch 2f\n"
               "1:\n\ts_getpc_b64 %0\n\ts_andn2_b64 %0, %0, 0xff\n\ts_load_dword %1, %0, %5\n\ts_load_dword %1, %0, %5 offset:64\n\ts_load_dword %1, %0, %5 offset:128\n2:"
               : "=&s"(tp), "=&s"(dump)
               : "s"(__builtin_amdgcn_kernarg_segment_ptr()), "s"(s * 8), "s"(wave),
                 "s"(min((u32)(((b >> 3) * 3 + (wave - 5)) * 192), (u32)(CODE_BYTES - 192))) : "scc");
  const u32 wof = (rbk * 144 + r8 * 18) * 8192 + (g * 256 + cb * 64) * 2 + (lane & 7) * 16;
  const int cbu = b < 36 ? b / 9 : 0;
  const u32 c2l = WS_C2 + cbu * 1024 + (lane & 3) * 256 + (lane & 8) * 1024, ga = WS_G + g * G_SIZE;
  u32 epo = lane > 4 && lane < 7 ? ga + G_C1 + rbk * 256 + (lane - 5) * 8192 : WS_EPOCH;
  epo = (lane & ~11) == (b < 36 ? 0 : 64) ? c2l : epo;
  const u32 pof = pi * 144 + n16 * 16;
  const u32 toff = g * (s < 4 ? (u32)SK * 256 : s < 6 ? 256u * 1152 : 0u);
  asm volatile("s_barrier\n\ts_waitcnt lgkmcnt(0)"
               : "+s"(A), "+s"(B), "+s"(C), "+s"(tp), "+s"(dump)
               : "s"(wave), "v"(wof), "v"(lane * 16), "s"(toff), "v"(epo), "v"(pof), "v"(lane * 2) : "memory");
  u64 t64;
  asm volatile("s_memrealtime %0" : "=s"(t64));
  SBAR();
  u32x4 w[18], xa, par;
  u32 xb, ep, b2v;
  if (wave == 2) {
    asm volatile("global_load_dwordx4 %0, %5, %9\n\tglobal_load_ushort %1, %6, %9 offset:1024\n\tglobal_load_dword %2, %7, %10 sc1\n\t"
                 "s_mov_b64 exec, 0x1ff\n\tglobal_load_dwordx4 %3, %8, %11\n\t"
                 "s_mov_b64 exec, 0x1ff0000\n\tglobal_load_dwordx4 %3, %8, %12\n\t"
                 "s_lshl_b64 exec, exec, 16\n\tglobal_load_dwordx4 %3, %8, %13\n\t"
                 "s_mov_b64 exec, %15\n\tglobal_load_ushort %4, %6, %14\n\ts_mov_b64 exec, -1"
                 : "=&v"(xa), "=&v"(xb), "=&v"(ep), "=&v"(par), "=&v"(b2v)
                 : "v"(lane * 16), "v"(lane * 2), "v"(epo), "v"(pof), "s"(A[1]), "s"(C), "s"(A[2]), "s"(A[3]), "s"(A[4]), "s"(b < 36 ? B[0] : A[1]), "s"(1ull)
                 : "scc", "memory");
  }
  if (wave == 4 && (s >= 6 || s < P_PT)) {
    asm volatile("s_load_dword %0, %1, 0x0" : "=&s"(dump) : "s"(tp + toff));
    }
  if (wave == 1) {
    const rsrc_t wqr = __builtin_amdgcn_make_buffer_rsrc((void*)A[0], 0, 576 * 8192, 0x00020000);
#pragma unroll
    for (int j = 0; j < 18; ++j) { w[j] = __builtin_amdgcn_raw_buffer_load_b128(wqr, wof, j * 8192, 2); SBAR(); }
  }
  SBAR();
  char* const ws = (char*)C;
  const rsrc_t wsr = __builtin_amdgcn_make_buffer_rsrc(ws, 0, WS_END, 0x00020000);
  size_t kvoff = ((size_t)g * SK + (SK - WIN)) * 256 + (s << 13);
  const char* wb = (const char*)(A[7] + ((g * 256 + rbk * 32) * 576 + cb * 144) * 2);
  asm volatile("s_waitcnt lgkmcnt(0)" : "+s"(t64), "+s"(dump));
  u32 t1 = (u32)t64;
  if (wave == 1) {
    asm volatile("s_memrealtime s[60:61]\n\ts_waitcnt lgkmcnt(0)\n\ts_sub_u32 %0, s60, %1\n\ts_or_b32 %0, %0, 1\n\tv_mov_b32 v200, %2\n\tv_mov_b32 v201, %0\n\tds_write_b32 v200, v201"
                 : "=s"(t1) : "s"(P_ROT), "s"(LB + F_T1) : "s60", "s61", "v200", "v201", "scc", "memory");
  } else if (wave == 0 || wave == 3) {
    flag_wait<0>(L + F_T1, 1);
    t1 = flag_ld(L + F_T1);
  }
  if (wave == 4) {
    wait_until<2>(t1 + P_MODE);
    u32 d0, d1, d2, d3;
    asm volatile("s_load_dword %0, %4, 0x0\n\ts_load_dword %1, %5, 0x0\n\ts_load_dword %2, %6, 0x0\n\ts_load_dword %3, %6, 0x24000\n\ts_waitcnt lgkmcnt(0)"
                 : "=&s"(d0), "=&s"(d1), "=&s"(d2), "=&s"(d3) : "s"(tp + toff), "s"(A[6] + kvoff), "s"(wb));
  }
  SBAR();
  STAMP(1);

  if (wave == 0 || wave == 3) {
    const bool isv = wave == 0;
    const u32 fa = LB + (isv ? F_PROG : F_KP);
    if (isv) {
      if (b < 8) st_unit(wsr, (lane >> 3) * 1024 + (lane & 7) * 16, WS_T0 + b * 128, asf(t1), asf(t1), ~t1);
      {
        u32 x0, x1, first;
        asm volatile("s_mov_b32 %1, 0x1badc0de\n\ts_load_dword %0, %3, %4\n\ts_load_dword %1, %3, %5\n\ts_waitcnt lgkmcnt(1)\n\ts_sleep 2\n\t"
                     "s_cmp_eq_u32 %1, 0x1badc0de\n\ts_cselect_b32 %2, 0, 1\n\ts_waitcnt lgkmcnt(0)"
                     : "=&s"(x0), "=&s"(x1), "=&s"(first) : "s"(C + (ga + G_PR + s * 256)), "s"((s & 1) * 8192), "s"(8192 - (s & 1) * 8192) : "scc", "memory");
        flag_st(L + F_V, (first ^ s) << 31);
      }
#pragma nounroll
      for (int pass = 0; pass < 2; ++pass) {
        wait_until<4>(t1 + (pass ? P_TR : P_WOA2));
        const u32x4 ot = ld_unit(wsr, (lane & 7) * 128, WS_T0 + (b & 7) * 1024);
        const int d = (int)(ot.x - t1);
        const bool valid = ot.y == ~ot.x && d > -256 && d < 256;
        const float sum = row_sum(valid ? (float)d : 0.f), cnt = row_sum(valid ? 1.f : 0.f);
        float mxd = valid ? (float)d : 0.f, zz = 0.f;
        row_max2(mxd, zz);
        const float mean = sum * __builtin_amdgcn_rcpf(fmaxf(cnt, 1.f));
        flag_st(L + F_SH, (u32)(int)(mean + (mxd - mean) * (P_BETA * 0.01f)));
        if (__builtin_amdgcn_readfirstlane(cnt > 15.f)) break;
      }
      wait_until<4>(t1 + DUE(82) - 60);
    }
    __builtin_amdgcn_s_setprio(3);
    load_tile(isv ? 82 : 18, LB + (isv ? L_V : L_K), (isv ? A[6] : A[5]) + kvoff, g, kg * 256 + ((n16 ^ (isv ? kg << 2 : kg)) << 4), isv ? 16 : 64, t1 + (isv ? P_KV : 0), fa, L + F_SH DBG_PASS);
    STAMP(3);
    u32 hv1 = lane < 4 ? ga + G_C1 + lane * 256 : WS_C2 + cb * 1024 + (g & 3) * 256 + (lane & 1) * 8192, hm = lane < 4 ? 8192 : 0;
    asm volatile("" : "+v"(hv1), "+v"(hm));
    drain<40, 8>(fa);
    if (isv) {
      STAMP(6);
      static_assert(F_PV == F_PI + 12, "");
      u32 n;
      asm volatile("s_mov_b32 %0, %6\n1:\n\tds_read2_b32 v[200:201], %1 offset0:3 offset1:0\n\ts_waitcnt lgkmcnt(0)\n\tv_cmp_le_u32 vcc, 4, v200\n\t"
                   "s_cbranch_vccnz 2f\n\ts_sub_u32 %0, %0, 1\n\ts_cbranch_scc0 1b\n"
                   "2:\n\tv_and_or_b32 v201, v201, %3, %2\n\ts_mov_b64 exec, 63\n\tbuffer_atomic_add %4, v201, %5, 0 offen\n\ts_mov_b64 exec, -1"
                   : "=&s"(n) : "v"(L + F_PI), "v"(hv1), "v"(hm), "v"(1u), "s"(wsr), "i"(SPIN_L) : "v200", "v201", "vcc", "scc", "memory");
    }
  }
  asm volatile("" ::: "memory");
  if (wave == 0 || wave == 3) {
    if (wave == 0) {
      STAMP(5);
    const u32 c2o = WS_C2 + cb * 1024 + (g & 3) * 256;
      flag_wait<1>(L + F_W, 1);
      {
        u32 t, n;
        asm volatile("s_mov_b32 %1, %7\n1:\n\tds_read_b32 %0, %2\n\ts_memrealtime s[60:61]\n\ts_waitcnt lgkmcnt(0)\n\tv_cmp_le_u32 vcc, 1, %0\n\t"
                     "s_cbranch_vccnz 2f\n\ts_sub_u32 %1, %1, 1\n\ts_cbranch_scc0 1b\n"
                     "2:\n\ts_mov_b64 exec, 3\n\tv_add_u32 %0, s60, %6\n\tbuffer_atomic_umax %0, %8, %4, %5 offen offset:4\n\tbuffer_atomic_add %3, %8, %4, %5 offen\n\ts_mov_b64 exec, -1"
                     : "=&v"(t), "=&s"(n) : "v"(L + F_O), "v"(0x10000u), "s"(wsr), "s"(c2o), "v"(P_H2D), "i"(SPIN_L), "v"(lane * 8192)
                     : "s60", "s61", "vcc", "scc", "memory");
      }
      STAMP(4);
      STAMP_OUT();
      return;
    }
    STAMP(2);
    flag_wait<2>(L + F_XN, 2);
    const u32 tag = flag_ld(L + F_TAG);
    u32 wov[9];
#pragma unroll
    for (int i = 0; i < 9; ++i) {
      const u32 e = i * 64 + lane, rs = (e * 3641) >> 16;
      wov[i] = e * 16 + rs * (1152 - 288);
      asm volatile("" : "+v"(wov[i]));
    }
    const u32 hv = lane * 16, h2s = WS_H2 + (cb * 9 + 8) * 16384 + pi * 128;
    const lptr ow = L + L_O + ((lane & 32) + ((lane >> 2) & 3) * 8 + ((lane >> 4) & 1) * 4 + (lane & 3)) * 2;
    const u32 h8v = (lane & 40) == 0 ? (lane >> 4) * 8192 + (lane & 7) * 16 : 0xfffffff0u;
    const lptr wa = L + L_WO + (4 * kg + (n16 >> 2)) * 288 + (lane & 3) * 8;
    asm volatile("" ::"s"(h2s), "v"(hv), "v"(ow), "v"(h8v), "v"(wa));
    flag_wait<2>(L + F_PROG, 20);
    const u32 adj = flag_ld(L + F_SH);
    flag_wait<2>(L + F_PI, 1);
    const u32 near = flag_ld(L + F_PI) & 8192, hbase = flag_ld(L + F_B1 + (near >> 11));
    const u32 hb = ga + G_H1 + rbk * 16384 + near, c1s = ga + G_C1 + rbk * 256 + near;
    asm volatile("" ::"s"(hb), "s"(hb + 4096), "s"(c1s), "s"(hbase));
    flag_wait<0>(L + F_PROG, P_WOA);
#pragma nounroll
    for (int hf = 0; hf < 2; ++hf) {
      if (hf) flag_wait<0>(L + F_PROG, P_WOB);
#pragma unroll
      for (int i = 0; i < 9; ++i) {
        if (i % 3 == 0) wait_until<0>(t1 + DUE(146) + P_KV + adj + ((P_BD >> (5 * (3 * hf + i / 3))) & 31) * 3);
        dma1(LB + L_WO + (hf * 9 + i) * 1024, wb + hf * (128 * 1152), wov[i]);
      }
    }
    flag_wait<0>(L + F_PV, 8);
    if (P_H1D) wait_until<0>(now() + P_H1D);
    hint_wait<1, 0>(wsr, 0u, c1s, hbase);
    u32x4 b80, b81;
#pragma nounroll
    for (u32 it = 0; it < SPIN_G; ++it) {
      STAMP(4);
      u32x4 u[8];
      sweep8_async(u, wsr, hv, hb);
      flag_st(L + F_O, 1);
      b80 = cat(trd(wa + 256), trd(wa + 16 * 288 + 256));
      b81 = cat(trd(wa + 32 * 288 + 256), trd(wa + 48 * 288 + 256));
      SBAR();
      flag_wait<0>(L + F_W, 1);
      f32x4 wt[4];
#pragma unroll
      for (int i = 0; i < 4; ++i) wt[i] = lds_ld<f32x4>(L + L_W + (lane >> 5) * 64 + i * 16);
      u32 d = 0, e = 0;
      asm volatile("" : "+v"(wt[0]), "+v"(wt[1]), "+v"(wt[2]), "+v"(wt[3]));
      STAMP(5);
      SBAR();
      float o0 = 0.f, o1 = 0.f;
#define H1STEP(i) asm volatile("s_waitcnt vmcnt(%1)" : "+v"(u[i]) : "i"(7 - i) : "memory"); \
      o0 = fmaf(wt[i >> 1][(i & 1) * 2], asf(u[i].x), o0); o1 = fmaf(wt[i >> 1][(i & 1) * 2 + 1], asf(u[i].z), o1); \
      d = __builtin_amdgcn_bitop3_b32(u[i].y, tag, d, 0xBE); e = __builtin_amdgcn_bitop3_b32(u[i].w, tag, e, 0xBE); SBAR();
      H1STEP(0) H1STEP(1) H1STEP(2) H1STEP(3) H1STEP(4) H1STEP(5) H1STEP(6) H1STEP(7)
      lds_st<__bf16>(ow, (__bf16)swap32_sum(o0, o1));
      const bool ok = __builtin_amdgcn_ballot_w64((d | e) != 0) == 0;
      flag_st(L + F_A, ok);
      STAMP(6);
      if (ok) break;
    }
    {
      const u32x4 a0 = lds_ld<u32x4>(L + L_O + kg * 16), a1 = lds_ld<u32x4>(L + L_O + 64 + kg * 16);
      const f32x4 c = mfma(a1, b81, mfma(a0, b80, f32x4{0.f, 0.f, 0.f, 0.f}));
      st_unit(wsr, h8v, h2s, c[0], dpp<ROR8>(c[0]), tag);
    }
    STAMP(7);
    STAMP_OUT();
    return;
  }

  if (wave == 1) {
    __builtin_amdgcn_s_setprio(3);
    flag_wait<0>(L + F_XN, 1);
    u32 xp[9];
#pragma unroll
    for (int i = 0; i < 9; ++i) xp[i] = lds_ld<u32>(L + L_XN + rbk * 288 + r8 * 36 + i * 4);
    float acc[8];
#pragma unroll
    for (int i = 0; i < 8; ++i) acc[i] = 0.f;
#pragma unroll
    for (int i = 0; i < 9; ++i)
#pragma unroll
      for (int d = 0; d < 4; ++d) {
        const u32 a = w[2 * i][d], c = w[2 * i + 1][d];
        acc[2 * d] = dot2(__builtin_amdgcn_perm(c, a, 0x05040100), xp[i], acc[2 * d]);
        acc[2 * d + 1] = dot2(__builtin_amdgcn_perm(c, a, 0x07060302), xp[i], acc[2 * d + 1]);
      }
    float t4[4], t2[2];
#pragma unroll
    for (int i = 0; i < 4; ++i) t4[i] = swap32_sum(acc[i], acc[i + 4]);
#pragma unroll
    for (int i = 0; i < 2; ++i) { t2[i] = swap16_sum(t4[i], t4[i + 2]); t2[i] += dpp<ROR8>(t2[i]); }
    const u32 qxb = WS_QX + g * 8192;
    flag_wait<0>(L + F_XN, 2);
    const u32 tag = flag_ld(L + F_TAG);
    u32 tq = t1 + P_QT;
    {
      const u32 tn = now();
      if ((int)(tn + QPD - tq) > 0) {
#pragma nounroll
        for (int k = 0; k < 64 && (int)(tn + QPD - tq) > 0; ++k) tq += P_QF;
        wait_until<0>(tq - QPD);
      }
    }
    if (!(lane & 8))
      __builtin_amdgcn_raw_buffer_store_b128(u32x4{asu(t2[0]), tag, asu(t2[1]), tag ^ flag_ld(L + F_V)}, wsr, (kg * 8 + (lane & 7)) * 16, qxb + rbk * 2048 + cb * 512, 16);
    STAMP(3);
    u32x4 u[8];
#pragma nounroll
    for (u32 it = 0; it < SPIN_G; ++it) {
      wait_until<1>(tq);
      tq += P_QF;
      STAMP(7);
      sweep8(u, wsr, lane * 16, qxb);
      u32 d = 0, e = 0;
#pragma unroll
      for (int i = 0; i < 8; ++i) { d = __builtin_amdgcn_bitop3_b32(u[i].y, tag, d, 0xBE); e = __builtin_amdgcn_bitop3_b32(u[i].w, tag, e, 0xBE); }
      if (__builtin_amdgcn_ballot_w64((d | (e << 1)) != 0) == 0) break;
    }
    STAMP(4);
#pragma unroll
    for (int h = 0; h < 2; ++h) {
      const float q0 = (asf(u[h].x) + asf(u[2 + h].x)) + (asf(u[4 + h].x) + asf(u[6 + h].x));
      const float q1 = (asf(u[h].z) + asf(u[2 + h].z)) + (asf(u[4 + h].z) + asf(u[6 + h].z));
      lds_st<u32>(L + L_Q + h * 256 + ((lane & 32) + (lane & 7) * 4 + ((lane >> 3) & 3)) * 4, pk2(q0, q1));
    }
    flag_st(L + F_Q, 1);
    u32 votes = 0;
#pragma unroll
    for (int i = 0; i < 8; ++i) votes += (u[i].w ^ tag) >> 31;
    const u32 near = NUMA && __builtin_amdgcn_readlane(votes, 0) + __builtin_amdgcn_readlane(votes, 32) > 8 ? 8192 : 0;
    flag_st(L + F_PI, near + 1);
    STAMP(5);
    flag_wait<0>(L + F_P, 4);
    {
      const u32x4 pv = lds_ld<u32x4>(L + L_P + lane * 16);
      const f32x4 m4 = lds_ld<f32x4>(L + L_ML + (lane & 32));
      const float mo = lds_ld<float>(L + L_ML + (lane & 32) + (r8 & 3) * 4);
      const float M = fmaxf(fmaxf(m4[0], m4[1]), fmaxf(m4[2], m4[3]));
      float l = dot2(pv[0], 0x3f803f80u, dot2(pv[1], 0x3f803f80u, 0.f)) + dot2(pv[2], 0x3f803f80u, dot2(pv[3], 0x3f803f80u, 0.f));
      l = row_sum(l * __builtin_amdgcn_exp2f((mo - M) * LOG2E));
      l = swap16_sum(l, l);
      if (!(lane & 28)) st_unit(wsr, (lane >> 5) * 64 + (lane & 3) * 16, ga + G_ST + near + s * 128, M, l, tag);
    }
    STAMP(6);
    flag_wait<1>(L + F_PV, P_QB);
#pragma nounroll
    for (u32 it = 0; it < SPIN_G; ++it) {
      CBAR();
      const u32x4 s0 = ld_unit(wsr, n16 * 128, ga + G_ST + near), s1 = ld_unit(wsr, n16 * 128 + 64, ga + G_ST + near);
      float M0 = asf(s0.x), M1 = asf(s1.x);
      row_max2(M0, M1);
      const float e0 = __builtin_amdgcn_exp2f((asf(s0.x) - M0) * LOG2E), e1 = __builtin_amdgcn_exp2f((asf(s1.x) - M1) * LOG2E);
      lds_st<f32x2>(L + L_W + (n16 & 1) * 64 + (n16 >> 1) * 8,
                    f32x2{e0 * __builtin_amdgcn_rcpf(row_sum(e0 * asf(s0.z))), e1 * __builtin_amdgcn_rcpf(row_sum(e1 * asf(s1.z)))});
      if (__builtin_amdgcn_ballot_w64(bad_tags(bad_tags(0u, s0, tag), s1, tag) != 0) == 0) break;
      __builtin_amdgcn_s_sleep(8);
    }
    flag_st(L + F_W, 1);
    if (b < 36) {
      const u32 hb2 = lds_ld<u32>(L + L_G + (lane & 3) * 4 + (near >> 8));
      const float b2f = lo16(lds_ld<u32>(L + L_G + 16));
      hint_wait<15, 0>(wsr, lane * 256, WS_C2 + cbu * 1024 + near, hb2);
      flag_wait<0>(L + F_O, 1);
#pragma nounroll
      for (u32 it = 0; it < SPIN_G; ++it) {
        sweep8(u, wsr, lane * 16, WS_Y + b * 8192);
        if (ok8(u, 0u, tag)) break;
      }
      lds_st<float>(L + L_G + 64 + ((lane & 7) + (lane >> 5) * 8) * 4, gate_of(sum64(u), b2f));
      flag_st(L + F_G, 1);
      if (b == 0 && lane < 8) st_unit(wsr, lane * 16, WS_EPOCH, asf(tag), asf(tag), tag);
    }
    STAMP_OUT();
    return;
  }

  if (wave == 2) {
    __builtin_amdgcn_s_setprio(3);
    asm volatile("s_waitcnt vmcnt(5)" : "+v"(xa), "+v"(xb)::"memory");
    float xv[9];
#pragma unroll
    for (int i = 0; i < 4; ++i) { xv[2 * i] = lo16(xa[i]); xv[2 * i + 1] = hi16(xa[i]); }
    xv[8] = lo16(xb);
    lds_st<u32x4>(L + L_X + lane * 16, xa);
    lds_st<u16>(L + L_X + 1024 + lane * 2, (u16)xb);
#pragma nounroll
    for (int pass = 0; pass < 2; ++pass) {
      asm volatile("" : "+s"(pass));
      ln9(xv, xv);
    const lptr dst = L + L_XN + pass * (L_H - L_XN);
      lds_st<u32x4>(dst + lane * 16, u32x4{pk2(xv[0], xv[1]), pk2(xv[2], xv[3]), pk2(xv[4], xv[5]), pk2(xv[6], xv[7])});
      lds_st<__bf16>(dst + 1024 + lane * 2, (__bf16)xv[8]);
      flag_st(L + F_XN, 1 + pass);
      if (pass == 0) {
        asm volatile("s_waitcnt vmcnt(4)" : "+v"(ep)::"memory");
        lds_st<u32>(L + F_TAG, __builtin_amdgcn_readlane(ep, 4) + 1u);
        lds_st<u32x2>(L + F_B1, u32x2{(u32)__builtin_amdgcn_readlane(ep, 5), (u32)__builtin_amdgcn_readlane(ep, 6)});
        flag_st(L + F_XN, 2);
      }
    }
    STAMP(2);
    asm volatile("s_waitcnt vmcnt(0)" : "+v"(b2v), "+v"(par)::"memory");
    lds_st<u32x4>(L + L_PAR + lane * 16, par);
    if (lane < 32) lds_st<u32>(L + L_Z + lane * 4, 0u);
    if (lane < 12) lds_st<u32>(L + L_G + lane * 4, ep);
    if (lane == 0) lds_st<u32>(L + L_G + 16, b2v);
    flag_st(L + F_XN, 3);
    if (b < 36 && b == cbu * 9) {
      u64 sv;
      asm volatile("s_mov_b64 %0, exec\n\ts_mov_b64 exec, 0xff\n\tbuffer_atomic_swap %1, %2, %3, %4 offen offset:4\n\ts_mov_b64 exec, %0"
                   : "=&s"(sv) : "v"(t1), "v"((lane & 3) * 256 + (lane & 4) * 2048), "s"(wsr), "s"(WS_C2 + cbu * 1024) : "memory");
    }
    __builtin_amdgcn_s_setprio(0);
  }

  flag_wait<2>(L + F_XN, 3);
  if (wave != 2) STAMP(1);
  const u32 tag = flag_ld(L + F_TAG);
  const int wv = wave - 4;
  float ya0 = 0.f, ya1 = 0.f;
  {
    u32 p1[9], pb[9], p2[9];
    gate_par(L, kg, p1, pb, p2);
    const lptr hp = L + L_H + (cb * 144 + (wave == 2 ? 112 : wv * 32) + n16) * 2;
#pragma nounroll
    for (int t = 1 - (wave >> 2); t < 2; ++t) {
      asm volatile("" : "+s"(t));
      ya0 = ya1;
      ya1 = gate_tile(lo16(lds_ld<u16>(hp + t * 32)), p1, pb, p2);
    }
  }

  if (wave == 2) {
    const int uu = (b - cbu * 9) * 8 + (lane & 7), hi = lane >> 5;
    const int col = b < 36 ? cbu * 144 + (uu < 64 ? (uu >> 4) * 32 + (uu & 15) + hi * 16 : 128 + (uu - 64) + hi * 8) : 0;
    const float xc = lo16(lds_ld<u16>(L + L_X + col * 2));
    const float yn = dpp<ROR8>(ya1);
    flag_wait<4>(L + F_PROG, 64);
    if (lane < 8) st_unit(wsr, lane * 16, WS_Y + (cb * 9 + 8) * 8192 + pi * 128, ya1, yn, tag);
    if (b < 36) {
      __builtin_amdgcn_s_setprio(3);
      u32 ov = (lane & 24) ? 0xfffffff0u : col * 2, sv16 = lane * 16;
      flag_wait<1>(L + F_PI, 1);
      const u32 near = flag_ld(L + F_PI) & 8192, h2b = WS_H2 + b * 16384 + near;
      const u32 hb2 = lds_ld<u32>(L + L_G + (lane & 3) * 4 + (near >> 8));
      asm volatile("" : "+v"(ov), "+v"(sv16) : "v"(xc), "s"(h2b), "s"(h2b + 4096));
      flag_wait<1>(L + F_O, 1);
      STAMP(4);
      const u32 tm = hint_wait_time(wsr, lane * 256, WS_C2 + cbu * 1024 + near, hb2);
      STAMP(3);
      wait_until<0>(tm - t1 < 4096u ? tm : t1);
      STAMP(7);
#pragma nounroll
      for (u32 it = 0; it < SPIN_G; ++it) {
        u32x4 u[8];
        sweep8_async(u, wsr, sv16, h2b);
        flag_wait<0>(L + F_G, 1);
        float gate = lds_ld<float>(L + L_G + 64 + ((lane & 7) + hi * 8) * 4);
        asm volatile("" : "+v"(gate));
        STAMP(5);
        float a0 = 0.f, a1 = 0.f;
        u32 d = 0, e = 0;
#define H2STEP(i) asm volatile("s_waitcnt vmcnt(%1)" : "+v"(u[i]) : "i"(7 - i) : "memory"); a0 += asf(u[i].x); a1 += asf(u[i].z); \
        d = __builtin_amdgcn_bitop3_b32(u[i].y, tag, d, 0xBE); e = __builtin_amdgcn_bitop3_b32(u[i].w, tag, e, 0xBE); SBAR();
        H2STEP(0) H2STEP(1) H2STEP(2) H2STEP(3) H2STEP(4) H2STEP(5) H2STEP(6) H2STEP(7)
        const u64 bad = __builtin_amdgcn_ballot_w64((d | e) != 0);
        const bool ok = bad == 0;
        float tot = swap32_sum(a0, a1);
        tot = swap16_sum(tot, tot);
        tot = rb(tot + dpp<ROR8>(tot));
        const u16 res = __builtin_bit_cast(u16, (__bf16)(xc + rb(tot * gate)));
        __builtin_amdgcn_raw_buffer_store_b16(res, __builtin_amdgcn_make_buffer_rsrc((void*)B[1], 0, ok ? 1152 : 0, 0x00020000), ov, 0, OUTAUX);
        asm volatile("s_cmp_eq_u64 %0, 0\n\ts_cbranch_scc0 1f\n\ts_endpgm\n1:" ::"s"(bad) : "scc", "memory");
        if (ok) break;
        asm volatile("s_waitcnt vmcnt(0)" ::: "memory");
      }
      STAMP(6);
    }
    STAMP_OUT();
    return;
  }

  {
    STAMP(2);
    __builtin_amdgcn_s_setprio(2);
    flag_wait<1>(L + F_KP, 16 * wv + 16);
    u32x4 kf[4][4];
#pragma unroll
    for (int t = 0; t < 4; ++t) {
      const lptr ka = L + L_K + wv * 16384 + n16 * 256 + (((4 * t + kg) ^ n16) << 4);
#pragma unroll
      for (int T = 0; T < 4; ++T) kf[T][t] = lds_ld<u32x4>(ka + T * 4096);
    }
    const int q4 = n16 >> 2, p4 = lane & 3;
    lptr va[2][2];
#pragma unroll
    for (int t = 0; t < 2; ++t)
#pragma unroll
      for (int nt = 0; nt < 2; ++nt)
        va[nt][t] = L + L_V + (8 * kg + 4 * t + q4) * 256 + (((2 * (2 * wv + nt) + (p4 >> 1)) ^ ((q4 << 2) | ((2 * kg + t) & 3))) << 4) + (p4 & 1) * 8;
    lptr pa[4];
#pragma unroll
    for (int sw = 0; sw < 4; ++sw) pa[sw] = L + (q4 == sw ? L_P + (n16 & 1) * 512 + sw * 128 : L_Z) + kg * 16;
    lptr pw = L + L_P + (lane >> 5) * 512 + wv * 128 + (lane & 31) * 2, mw = L + L_ML + (lane & 32) + wv * 4, mo = L + L_ML + kg * 4;
    const u32 h1s = ga + G_H1 + wv * 16384 + s * 512, ys = WS_Y + (cb * 9 + wv * 2) * 8192 + pi * 128;
    u32 h1v = lane * 16, yv = (lane >> 3) * 8192 + (lane & 7) * 16;
    asm volatile("" : "+v"(pa[0]), "+v"(pa[1]), "+v"(pa[2]), "+v"(pa[3]), "+v"(pw), "+v"(mw), "+v"(mo), "+v"(h1v), "+v"(yv)
                 : "v"(va[0][0]), "v"(va[0][1]), "v"(va[1][0]), "v"(va[1][1]), "s"(h1s), "s"(ys));
    SBAR();
    STAMP(3);
    u32x4 qf[4];
    static_assert(F_Q == F_PROG + 12, "");
    const u32 seen = q_wait(qf, L + L_Q + (n16 & 1) * 256 + kg * 16, L + F_PROG);
    const f32x4 z4 = {0.f, 0.f, 0.f, 0.f};
    f32x4 sc[4];
#pragma unroll
    for (int T = 0; T < 4; ++T) sc[T] = mfma(qf[0], kf[T][0], z4);
#pragma unroll
    for (int t = 1; t < 4; ++t)
#pragma unroll
      for (int T = 0; T < 4; ++T) sc[T] = mfma(qf[t], kf[T][t], sc[T]);
    u32 r = pk2(sel4(kg, sc[0][0], sc[1][0], sc[2][0], sc[3][0]), sel4(kg, sc[0][1], sc[1][1], sc[2][1], sc[3][1]));
    r = pk2(lo16(r) * 0.08838834764831845f, hi16(r) * 0.08838834764831845f);
    const auto sw2 = __builtin_amdgcn_permlane32_swap(r << 16, r & 0xffff0000u, false, false);
    const float sa = asf(sw2[0]), sb2 = asf(sw2[1]);
    const float mx = half_max(vmax(sa, sb2));
    const float ea = __builtin_amdgcn_exp2f((sa - mx) * LOG2E), eb = __builtin_amdgcn_exp2f((sb2 - mx) * LOG2E);
    asm volatile("ds_write_b16 %0, %1\n\tds_write_b16_d16_hi %0, %1 offset:64\n\tds_write_b32 %2, %3" ::"v"(pw), "v"(pk2(ea, eb)), "v"(mw), "v"(mx) : "memory");
    flag_add(LB + F_P);
    STAMP(4);
    u32x2 x0[16], x4[8], x6[4], x7[4];
    v_early<0>(x0, va, seen);
    v_early<4>(x4, va, seen);
    v_early<6>(x6, va, seen);
    v_early<7>(x7, va, seen);
    u32x4 pf[8];
    f32x4 m40, m41;
    f32x2 own;
    p_wait(pf, m40, m41, own, pa, L + L_ML, mo, L + F_P);
    STAMP(5);
    flag_wait<0>(L + F_PI, 1);
    h1v += lds_ld<u32>(L + F_PI) & 8192;
    asm volatile("" : "+v"(h1v));
    v_late<0>(x0, va, seen, L + F_PROG);
    f32x4 c0 = z4, c1 = z4;
#pragma unroll
    for (int ks = 0; ks < 4; ++ks) { c0 = mfma(pf[ks], cat2(x0[4 * ks], x0[4 * ks + 1]), c0); c1 = mfma(pf[ks], cat2(x0[4 * ks + 2], x0[4 * ks + 3]), c1); }
    float al0 = __builtin_amdgcn_exp2f((own.x - fmaxf(fmaxf(m40[0], m40[1]), fmaxf(m40[2], m40[3]))) * LOG2E);
    float al1 = __builtin_amdgcn_exp2f((own.y - fmaxf(fmaxf(m41[0], m41[1]), fmaxf(m41[2], m41[3]))) * LOG2E);
    asm volatile("" : "+v"(al0), "+v"(al1));
    SBAR();
    v_late<4>(x4, va, seen, L + F_PROG);
#pragma unroll
    for (int ks = 0; ks < 2; ++ks) { c0 = mfma(pf[4 + ks], cat2(x4[4 * ks], x4[4 * ks + 1]), c0); c1 = mfma(pf[4 + ks], cat2(x4[4 * ks + 2], x4[4 * ks + 3]), c1); }
    SBAR();
    STAMP(6);
    v_late<6>(x6, va, seen, L + F_PROG);
    c0 = mfma(pf[6], cat2(x6[0], x6[1]), c0); c1 = mfma(pf[6], cat2(x6[2], x6[3]), c1);
    SBAR();
    flag_add(LB + F_PV);
    v_late<7>(x7, va, seen, L + F_PROG);
    c0 = mfma(pf[7], cat2(x7[0], x7[1]), c0); c1 = mfma(pf[7], cat2(x7[2], x7[3]), c1);
    float r0 = swap16_sum(c0[0] * al0, c1[0] * al0), r1 = swap16_sum(c0[1] * al1, c1[1] * al1);
    r0 = swap32_sum(r0, r0); r1 = swap32_sum(r1, r1);
    if (lane < 32) st_unit(wsr, h1v, h1s, r0, r1, tag);
    flag_add(LB + F_PV);
    SBAR();
    if (lane < 16) st_unit(wsr, yv, ys, ya0, ya1, tag);
    STAMP(7);
    const lptr wa = L + L_WO + (4 * kg + q4) * 288 + p4 * 8 + wv * 64;
    u32 h2v = lane < 32 ? ((lane >> 3) & 1) * 16384 + (lane >> 4) * 8192 + (lane & 7) * 16 : 0xfffffff0u;
    const u32 h2s = WS_H2 + (cb * 9 + 2 * wv) * 16384 + pi * 128;
    flag_wait<1>(L + F_O, 1);
    u32x4 bw[2][2];
#pragma unroll
    for (int nt = 0; nt < 2; ++nt)
#pragma unroll
      for (int ks = 0; ks < 2; ++ks) bw[nt][ks] = cat(trd(wa + 32 * ks * 288 + nt * 32), trd(wa + (32 * ks + 16) * 288 + nt * 32));
    asm volatile("" : "+v"(bw[0][0]), "+v"(bw[0][1]), "+v"(bw[1][0]), "+v"(bw[1][1]), "+v"(h2v) : "s"(h2s));
    u32x4 a0, a1;
    {
      u32 t, n;
      asm volatile("s_mov_b32 %3, %6\n1:\n\tds_read_b32 %2, %5\n\tds_read_b128 %0, %4\n\tds_read_b128 %1, %4 offset:64\n\t"
                   "s_waitcnt lgkmcnt(0)\n\tv_cmp_le_u32 vcc, 1, %2\n\ts_cbranch_vccnz 2f\n\ts_sub_u32 %3, %3, 1\n\ts_cbranch_scc0 1b\n2:"
                   : "=&v"(a0), "=&v"(a1), "=&v"(t), "=&s"(n) : "v"(L + L_O + kg * 16), "v"(L + F_A), "i"(SPIN_L) : "vcc", "scc", "memory");
    }
    c0 = mfma(a0, bw[0][0], z4); c1 = mfma(a0, bw[1][0], z4);
    c0 = mfma(a1, bw[0][1], c0); c1 = mfma(a1, bw[1][1], c1);
    st_unit(wsr, h2v, h2s, c0[0], c1[0], tag);
    STAMP(1);
    STAMP_OUT();
  }
}

extern "C" void kernel_launch(void* const* d_in, const int* in_sizes, int n_in, void* d_out, int out_size, void* d_ws, size_t ws_size,
                              hipStream_t stream) {
  const char *kp = (const char*)d_in[1], *vp = (const char*)d_in[2], *qp = (const char*)d_in[3], *op = (const char*)d_in[4];
  const size_t w0 = (size_t)(SK - WIN) * 256, w1 = (size_t)SK * 256 - 64;
  Touch t = {{kp + w0, kp + w1, vp + w0, vp + w1, op, op + 255 * 1152, qp, qp + (2 << 20), qp + (4 << 20), d_in[0], d_in[6], d_in[7], d_in[8],
                    d_out, (const char*)d_ws + 2048, (const char*)d_ws + WS_END}};
  hipLaunchKernelGGL(fused_decode, dim3(256), dim3(512), 0, stream, (const u16*)d_in[3], (const u16*)d_in[0], (const u16*)d_in[6],
                     (const u16*)d_in[7], (const u16*)d_in[8], (const u16*)d_in[1], (const u16*)d_in[2], (const u16*)d_in[4],
                     (const u16*)d_in[9], (u16*)d_out, (char*)d_ws, t);
}
```
